```python
import torch

def _softmax(x, axis=-1):
    return torch.softmax(x, dim=axis)
def _gelu(x):
    return torch.nn.functional.gelu(x, approximate="tanh")
def _silu(x):
    return torch.nn.functional.silu(x)
def _sigmoid(x):
    return torch.sigmoid(x)
def _layernorm(x, axis=-1, eps=1e-5):
    m = x.mean(dim=axis, keepdim=True)
    v = x.var(dim=axis, keepdim=True, unbiased=False)
    return (x - m) / torch.sqrt(v + eps)
def _scatter_add(tgt, idx, src, axis=0):
    return tgt.clone().index_add_(axis, idx, src)
def _segment_sum(src, seg, n):
    out = torch.zeros((n,) + src.shape[1:], dtype=src.dtype)
    return out.index_add_(0, seg, src)
def _pad(x, pw):
    flat = tuple(v for pair in reversed(pw) for v in pair)
    return torch.nn.functional.pad(x, flat)

kernel_name = 'sequential_preLN__sliding_local_dilated__glu_gated_swiglu__prompt'

def setup_inputs():

    _w = (lambda a: (lambda t: t.to(torch.bfloat16) if t.is_floating_point() else t)(torch.as_tensor(a)))
    def _A_setup():
        B, S, D = 1, 2048, 512
        return {
            "x": _w((lambda *s: torch.randn(*s, dtype=torch.bfloat16) * 0.05)(B, S, D)),
            "wqkv": _w((lambda *s: torch.randn(*s, dtype=torch.bfloat16) * 0.05)(D, 3 * D)),
            "wo": _w((lambda *s: torch.randn(*s, dtype=torch.bfloat16) * 0.05)(D, D)),
        }
    _A_in = _A_setup()

    def _B_setup():
        B, S, D, F = 1, 2048, 512, 1792
        return {
            "x": _w((lambda *s: torch.randn(*s, dtype=torch.bfloat16) * 0.05)(B, S, D)),
            "w1": _w((lambda *s: torch.randn(*s, dtype=torch.bfloat16) * 0.8)(D, F) * (2.0 / D) ** 0.5),
            "w3": _w((lambda *s: torch.randn(*s, dtype=torch.bfloat16) * 0.8)(D, F) * (2.0 / D) ** 0.5),
            "w2": _w((lambda *s: torch.randn(*s, dtype=torch.bfloat16) * 0.8)(F, D) * (2.0 / F) ** 0.5),
        }
    _B_in = _B_setup()

    return {
        "A_x": _A_in["x"],
        "A_wqkv": _A_in["wqkv"],
        "A_wo": _A_in["wo"],
        "B_x": _B_in["x"],
        "B_w1": _B_in["w1"],
        "B_w3": _B_in["w3"],
        "B_w2": _B_in["w2"],
    }

def reference(A_x, A_wqkv, A_wo, B_x, B_w1, B_w3, B_w2):
    def _A_ref(x, wqkv, wo):
        B, S, D = x.shape
        H = 32; Dh = D // H
        W, dil = 8, 2
        qkv = (lambda x, *d: x.permute(*d))((x @ wqkv).reshape(B, S, 3, H, Dh), 2, 0, 3, 1, 4)
        q, k, v = qkv[0], qkv[1], qkv[2]
        scale = Dh ** -0.5
        pos = torch.arange(S)
        off = torch.arange(W) * dil
        idx = pos[:, None] - off[None, :]
        mask = idx >= 0
        idx_c = torch.where(mask, idx, 0).reshape(-1)
        kg = k[:, :, idx_c, :].reshape(B, H, S, W, Dh)
        vg = v[:, :, idx_c, :].reshape(B, H, S, W, Dh)
        logits = torch.einsum("bhsd,bhswd->bhsw", q, kg) * scale
        logits = torch.where(mask[None, None, :, :], logits, -1e9)
        attn = _softmax(logits, axis=-1)
        o = torch.einsum("bhsw,bhswd->bhsd", attn, vg)
        return (lambda x, *d: x.permute(*d))(o, 0, 2, 1, 3).reshape(B, S, D) @ wo

    def _B_ref(x, w1, w3, w2):
        gate = torch.einsum("bsd,df->bsf", x, w1)
        up = torch.einsum("bsd,df->bsf", x, w3)
        h = _silu(gate) * up
        out = torch.einsum("bsf,fd->bsd", h, w2)
        return out

    x = A_x
    h = x + _A_ref(_layernorm(x, axis=-1), A_wqkv, A_wo)
    h2 = h + _B_ref(_layernorm(h, axis=-1), B_w1, B_w3, B_w2)
    return h2
```

```cpp
#include <hip/hip_runtime.h>
#include <hip/hip_bf16.h>
typedef unsigned short u16;
__device__ __forceinline__ float bf2f(u16 b){ return __uint_as_float(((unsigned)b)<<16); }
__device__ __forceinline__ u16 f2bf(float f){ unsigned u=__float_as_uint(f); u += 0x7FFFu + ((u>>16)&1u); return (u16)(u>>16); }
constexpr int S=2048, D=512, F=1792;
__global__ void k_ln(const u16* x, u16* y){
  int r=blockIdx.x; int t=threadIdx.x;
  float v[8]; float s=0;
  for(int i=0;i<8;i++){ v[i]=bf2f(x[r*D+t*8+i]); s+=v[i]; }
  for(int o=32;o;o>>=1) s+=__shfl_xor(s,o);
  float m=s/D; float q=0;
  for(int i=0;i<8;i++){ float d=v[i]-m; q+=d*d; }
  for(int o=32;o;o>>=1) q+=__shfl_xor(q,o);
  float rs=rsqrtf(q/D+1e-5f);
  for(int i=0;i<8;i++) y[r*D+t*8+i]=f2bf((v[i]-m)*rs);
}
template<int MODE>
__global__ void k_gemm(const u16* A, const u16* B, const u16* B2, const u16* res, u16* C, int N, int K){
  int n=blockIdx.x*64+(threadIdx.x&63); int m=blockIdx.y*4+(threadIdx.x>>6);
  float a=0,b=0;
  for(int k=0;k<K;k++){ float av=bf2f(A[m*K+k]); a+=av*bf2f(B[k*N+n]); if(MODE==2) b+=av*bf2f(B2[k*N+n]); }
  if(MODE==1) a+=bf2f(res[m*N+n]);
  if(MODE==2) a = a/(1.f+__expf(-a))*b;
  C[m*N+n]=f2bf(a);
}
__global__ void k_attn(const u16* qkv, u16* o){
  int s=blockIdx.x; int h=threadIdx.x; if(h>=32) return;
  float l[8]; float mx=-1e30f;
  for(int w=0;w<8;w++){ int p=s-2*w; if(p<0){l[w]=-1e9f;} else { float a=0; for(int d=0;d<16;d++) a+=bf2f(qkv[s*1536+h*16+d])*bf2f(qkv[p*1536+512+h*16+d]); l[w]=a*0.25f;} mx=fmaxf(mx,l[w]); }
  float sum=0; for(int w=0;w<8;w++){ l[w]=__expf(l[w]-mx); sum+=l[w]; }
  for(int d=0;d<16;d++){ float a=0; for(int w=0;w<8;w++){ int p=s-2*w; if(p<0)p=0; a+=l[w]*bf2f(qkv[p*1536+1024+h*16+d]); } o[s*512+h*16+d]=f2bf(a/sum); }
}
extern "C" void kernel_launch(void* const* d_in, const int* in_sizes, int n_in, void* d_out, int out_size, void* d_ws, size_t ws_size, hipStream_t stream) {
  const u16* x=(const u16*)d_in[0]; const u16* wqkv=(const u16*)d_in[1]; const u16* wo=(const u16*)d_in[2];
  const u16* w1=(const u16*)d_in[4]; const u16* w3=(const u16*)d_in[5]; const u16* w2=(const u16*)d_in[6];
  u16* ws=(u16*)d_ws; u16* xn=ws; u16* qkv=xn+S*D; u16* o=qkv+S*3*D; u16* h=o+S*D; u16* act=h+S*D; u16* out=(u16*)d_out;
  k_ln<<<S,64,0,stream>>>(x,xn);
  k_gemm<0><<<dim3(1536/64,S/4),256,0,stream>>>(xn,wqkv,wqkv,x,qkv,1536,512);
  k_attn<<<S,64,0,stream>>>(qkv,o);
  k_gemm<1><<<dim3(512/64,S/4),256,0,stream>>>(o,wo,wo,x,h,512,512);
  k_ln<<<S,64,0,stream>>>(h,xn);
  k_gemm<2><<<dim3(F/64,S/4),256,0,stream>>>(xn,w1,w3,x,act,F,512);
  k_gemm<1><<<dim3(512/64,S/4),256,0,stream>>>(act,w2,w2,h,out,512,F);
}
```

```cpp
#include <hip/hip_runtime.h>
#include <hip/hip_bf16.h>

__global__ __launch_bounds__(256, 1) void fused_block_kernel(const void* x, const void* wqkv, const void* wo, const void* w1,
                                                             const void* w3, const void* w2, void* out, void* ws) {
  __shared__ __attribute__((aligned(1024))) char smem[163840];
  asm volatile(
    "s_load_dwordx2 s[8:9], %[ka], 0\n\t"
    "s_load_dwordx2 s[12:13], %[ka], 8\n\t"
    "s_load_dwordx2 s[16:17], %[ka], 16\n\t"
    "s_load_dwordx2 s[20:21], %[ka], 24\n\t"
    "s_load_dwordx2 s[24:25], %[ka], 32\n\t"
    "s_load_dwordx2 s[28:29], %[ka], 56\n\t"
    "v_lshrrev_b32 v1, 6, v0\n\t"
    "s_mov_b32 s43, %[bid]\n\t"
    "v_readfirstlane_b32 s33, v1\n\t"
    "s_bfe_u32 s34, s43, 0x20001\n\t"
    "s_lshr_b32 s44, s43, 5\n\t"
    "v_and_b32 v11, 15, v0\n\t"
    "s_lshl3_add_u32 s47, s34, s44\n\t"
    "v_bfe_u32 v12, v0, 4, 2\n\t"
    "s_mul_i32 s39, s33, 20\n\t"
    "s_lshl_b32 s40, s47, 6\n\t"
    "s_add_u32 s39, s39, s40\n\t"
    "s_sub_u32 s39, s39, 16\n\t"
    "v_add_u32 v7, s39, v12\n\t"
    "s_mul_i32 s48, s33, 5120\n\t"
    "s_add_u32 s48, s48, 73728\n\t"
    "s_mov_b32 s10, 2097152\n\t"
    "s_mov_b32 s11, 0x20000\n\t"
    "s_lshl_b32 s58, s43, 3\n\t"
    "s_and_b32 s58, s58, 0x300\n\t"
    "s_cmp_eq_u32 s33, 0\n\t"
    "s_cselect_b32 s62, 0xffff0000, -1\n\t"
    "v_xor_b32 v8, v7, v11\n\t"
    "v_max_i32 v9, 0, v7\n\t"
    "v_and_b32 v8, 15, v8\n\t"
    "v_lshlrev_b32 v9, 10, v9\n\t"
    "v_lshl_add_u32 v2, v8, 4, v9\n\t"
    "s_add_u32 m0, s48, 0\n\t"
    "s_mov_b32 exec_lo, s62\n\t"
    "s_waitcnt lgkmcnt(0)\n\t"
    "buffer_load_dwordx4 v2, s[8:11], s58 offen offset:0 lds\n\t"
    "s_mov_b32 exec_lo, -1\n\t"
    "v_add_u32 v7, 4, v7\n\t"
    "v_xor_b32 v8, v7, v11\n\t"
    "v_add_u32 v9, -1, v7\n\t"
    "v_max_i32 v9, 0, v9\n\t"
    "v_and_b32 v8, 15, v8\n\t"
    "v_lshlrev_b32 v9, 10, v9\n\t"
    "v_lshl_add_u32 v3, v8, 4, v9\n\t"
    "buffer_load_dwordx4 v3, s[8:11], s58 offen offset:1024 lds\n\t"
    "v_add_u32 v7, 4, v7\n\t"
    "v_xor_b32 v8, v7, v11\n\t"
    "v_add_u32 v9, -2, v7\n\t"
    "v_max_i32 v9, 0, v9\n\t"
    "v_and_b32 v8, 15, v8\n\t"
    "v_lshlrev_b32 v9, 10, v9\n\t"
    "v_lshl_add_u32 v4, v8, 4, v9\n\t"
    "buffer_load_dwordx4 v4, s[8:11], s58 offen offset:2048 lds\n\t"
    "v_add_u32 v7, 4, v7\n\t"
    "v_xor_b32 v8, v7, v11\n\t"
    "v_add_u32 v9, -3, v7\n\t"
    "v_max_i32 v9, 0, v9\n\t"
    "v_and_b32 v8, 15, v8\n\t"
    "v_lshlrev_b32 v9, 10, v9\n\t"
    "v_lshl_add_u32 v5, v8, 4, v9\n\t"
    "buffer_load_dwordx4 v5, s[8:11], s58 offen offset:3072 lds\n\t"
    "v_add_u32 v7, 4, v7\n\t"
    "v_xor_b32 v8, v7, v11\n\t"
    "v_max_i32 v9, 0, v7\n\t"
    "v_and_b32 v8, 15, v8\n\t"
    "v_lshlrev_b32 v9, 10, v9\n\t"
    "v_lshl_add_u32 v6, v8, 4, v9\n\t"
    "s_add_u32 m0, s48, 4096\n\t"
    "s_nop 0\n\t"
    "buffer_load_dwordx4 v6, s[8:11], s58 offen offset:0 lds\n\t"
    "v_and_b32 v1, 63, v0\n\t"
    "s_and_b32 s35, s43, 1\n\t"
    "s_bfe_u32 s45, s43, 0x20003\n\t"
    "s_lshl2_add_u32 s46, s35, s45\n\t"
    "v_lshrrev_b32 v7, 3, v1\n\t"
    "s_lshl_b32 s39, s33, 4\n\t"
    "v_and_b32 v9, 2, v7\n\t"
    "v_and_b32 v8, 7, v1\n\t"
    "v_xor_b32 v8, v8, v9\n\t"
    "v_add_u32 v7, s39, v7\n\t"
    "v_mul_u32_u24 v7, 3072, v7\n\t"
    "s_lshl_b32 s39, s46, 7\n\t"
    "v_add_u32 v7, s39, v7\n\t"
    "v_lshl_add_u32 v13, v8, 4, v7\n\t"
    "v_xor_b32 v14, 64, v13\n\t"
    "v_add_u32 v14, 24576, v14\n\t"
    "s_mul_i32 s49, s33, 6144\n\t"
    "s_mov_b32 s14, 1572864\n\t"
    "s_mov_b32 s15, 0x20000\n\t"
    "s_lshr_b32 s36, s43, 3\n\t"
    "s_and_b32 s63, s43, 0\n\t"
    "s_add_u32 s39, s58, s63\n\t"
    "s_mul_i32 s50, s39, 1536\n\t"
    "s_xor_b32 s39, s39, 128\n\t"
    "s_mul_i32 s51, s39, 1536\n\t"
    "s_add_u32 m0, s49, 0\n\t"
    "s_nop 0\n\t"
    "buffer_load_dwordx4 v13, s[12:15], s50 offen offset:0 lds\n\t"
    "buffer_load_dwordx4 v13, s[12:15], s50 offen offset:1024 lds\n\t"
    "buffer_load_dwordx4 v13, s[12:15], s50 offen offset:2048 lds\n\t"
    "s_add_u32 m0, s49, 3072\n\t"
    "s_nop 0\n\t"
    "buffer_load_dwordx4 v14, s[12:15], s50 offen offset:0 lds\n\t"
    "buffer_load_dwordx4 v14, s[12:15], s50 offen offset:1024 lds\n\t"
    "buffer_load_dwordx4 v14, s[12:15], s50 offen offset:2048 lds\n\t"
    "s_add_u32 s59, s58, 256\n\t"
    "s_and_b32 s59, s59, 0x300\n\t"
    "s_add_u32 s39, s59, s63\n\t"
    "s_mul_i32 s52, s39, 1536\n\t"
    "s_xor_b32 s39, s39, 128\n\t"
    "s_mul_i32 s53, s39, 1536\n\t"
    "s_add_u32 s60, s59, 256\n\t"
    "s_and_b32 s60, s60, 0x300\n\t"
    "s_add_u32 s39, s60, s63\n\t"
    "s_mul_i32 s54, s39, 1536\n\t"
    "s_xor_b32 s39, s39, 128\n\t"
    "s_mul_i32 s55, s39, 1536\n\t"
    "s_add_u32 s61, s60, 256\n\t"
    "s_and_b32 s61, s61, 0x300\n\t"
    "s_add_u32 s39, s61, s63\n\t"
    "s_mul_i32 s56, s39, 1536\n\t"
    "s_xor_b32 s39, s39, 128\n\t"
    "s_mul_i32 s57, s39, 1536\n\t"
    "s_mov_b32 s18, 524288\n\t"
    "s_mov_b32 s19, 0x20000\n\t"
    "s_mov_b32 s22, 1835008\n\t"
    "s_mov_b32 s23, 0x20000\n\t"
    "s_mov_b32 s26, 1835008\n\t"
    "s_mov_b32 s27, 0x20000\n\t"
    "s_mov_b32 s30, 12582912\n\t"
    "s_mov_b32 s31, 0x20000\n\t"
    "v_mov_b32 v7, 0\n\t"
    "buffer_load_dword v16, v7, s[28:31], 0 offen sc1\n\t"
    "s_lshl_b32 s39, s33, 16\n\t"
    "s_mul_i32 s40, s43, 128\n\t"
    "s_add_u32 s39, s39, s40\n\t"
    "s_add_u32 s39, s39, 65536\n\t"
    "buffer_load_dword v15, v7, s[28:31], s39 offen sc1\n\t"
    "s_getpc_b64 s[64:65]\n\t"
    "L_pc_%=:\n\t"
    "s_and_b32 s65, s65, 0xffff\n\t"
    "s_mov_b32 s66, L_end_%=-L_pc_%=\n\t"
    "s_mov_b32 s67, 0x20000\n\t"
    "v_and_b32 v7, 3, v0\n\t"
    "v_lshlrev_b32 v7, 7, v7\n\t"
    "s_lshl2_add_u32 s39, s36, s33\n\t"
    "s_lshl_b32 s39, s39, 9\n\t"
    "v_add_u32 v7, s39, v7\n\t"
    "buffer_load_dword v15, v7, s[64:67], 0 offen\n\t"
    "s_lshr_b32 s39, s36, 3\n\t"
    "s_lshl2_add_u32 s39, s35, s39\n\t"
    "s_lshl3_add_u32 s64, s34, s39\n\t"
    "s_and_b32 s65, s36, 7\n\t"
    "v_and_b32 v17, 31, v1\n\t"
    "v_lshrrev_b32 v18, 5, v1\n\t"
    "v_lshlrev_b32 v19, 4, v1\n\t"
    "s_lshr_b32 s39, s33, 1\n\t"
    "s_lshl1_add_u32 s39, s64, s39\n\t"
    "s_lshl_b32 s39, s39, 15\n\t"
    "s_and_b32 s40, s33, 1\n\t"
    "s_lshl1_add_u32 s40, s65, s40\n\t"
    "s_lshl_b32 s40, s40, 6\n\t"
    "s_add_u32 s39, s39, s40\n\t"
    "v_lshrrev_b32 v7, 2, v1\n\t"
    "v_lshlrev_b32 v7, 10, v7\n\t"
    "v_and_b32 v8, 3, v1\n\t"
    "v_lshl_add_u32 v7, v8, 4, v7\n\t"
    "v_add_u32 v28, s39, v7\n\t"
    "v_xor_b32 v7, v12, v11\n\t"
    "v_lshlrev_b32 v7, 4, v7\n\t"
    "v_lshl_add_u32 v7, v11, 8, v7\n\t"
    "v_xor_b32 v7, s63, v7\n\t"
    "v_add_u32 v29, 73728, v7\n\t"
    "v_xor_b32 v30, 64, v29\n\t"
    "v_xor_b32 v31, 128, v29\n\t"
    "v_xor_b32 v32, 192, v29\n\t"
    "v_and_b32 v8, 1, v12\n\t"
    "v_bfe_u32 v9, v1, 2, 2\n\t"
    "v_mul_u32_u24 v7, 3072, v12\n\t"
    "v_lshl_add_u32 v7, v9, 7, v7\n\t"
    "v_lshrrev_b32 v9, 1, v9\n\t"
    "v_lshl_or_b32 v9, v8, 1, v9\n\t"
    "v_xor_b32 v9, s33, v9\n\t"
    "v_lshl_add_u32 v7, v9, 5, v7\n\t"
    "v_and_b32 v8, 3, v1\n\t"
    "v_lshl_add_u32 v33, v8, 3, v7\n\t"
    "v_add_u32 v34, 49152, v33\n\t"
    "v_add_u32 v35, s48, v19\n\t"
    "v_mov_b32 v36, 0x3f803f80\n\t"
    "v_mov_b32 v37, 0x3f803f80\n\t"
    "v_mov_b32 v38, 0x3f803f80\n\t"
    "v_mov_b32 v39, 0x3f803f80\n\t"
    "v_mul_u32_u24 v7, 20480, v12\n\t"
    "v_lshl_add_u32 v7, v11, 4, v7\n\t"
    "v_add_u32 v7, 73728, v7\n\t"
    "ds_write_b128 v7, v[36:39]\n\t"
    "s_add_u32 m0, s49, 24576\n\t"
    "s_nop 0\n\t"
    "buffer_load_dwordx4 v13, s[12:15], s51 offen offset:0 lds\n\t"
    "buffer_load_dwordx4 v13, s[12:15], s51 offen offset:1024 lds\n\t"
    "buffer_load_dwordx4 v13, s[12:15], s51 offen offset:2048 lds\n\t"
    "s_add_u32 m0, s49, 27648\n\t"
    "s_nop 0\n\t"
    "buffer_load_dwordx4 v14, s[12:15], s51 offen offset:0 lds\n\t"
    "buffer_load_dwordx4 v14, s[12:15], s51 offen offset:1024 lds\n\t"
    "buffer_load_dwordx4 v14, s[12:15], s51 offen offset:2048 lds\n\t"
    "s_waitcnt vmcnt(15)\n\t"
    "ds_read_b128 v[196:199], v35 offset:0\n\t"
    "ds_read_b128 v[200:203], v35 offset:1024\n\t"
    "ds_read_b128 v[204:207], v35 offset:2048\n\t"
    "ds_read_b128 v[208:211], v35 offset:3072\n\t"
    "ds_read_b128 v[212:215], v35 offset:4096\n\t"
    "s_waitcnt lgkmcnt(4)\n\t"
    "v_dot2_f32_bf16 v186, v196, v36, 0\n\t"
    "v_dot2_f32_bf16 v187, v196, v196, 0\n\t"
    "s_waitcnt lgkmcnt(3)\n\t"
    "v_dot2_f32_bf16 v188, v200, v36, 0\n\t"
    "v_dot2_f32_bf16 v189, v200, v200, 0\n\t"
    "s_waitcnt lgkmcnt(2)\n\t"
    "v_dot2_f32_bf16 v190, v204, v36, 0\n\t"
    "v_dot2_f32_bf16 v191, v204, v204, 0\n\t"
    "s_waitcnt lgkmcnt(1)\n\t"
    "v_dot2_f32_bf16 v192, v208, v36, 0\n\t"
    "v_dot2_f32_bf16 v193, v208, v208, 0\n\t"
    "s_waitcnt lgkmcnt(0)\n\t"
    "v_dot2_f32_bf16 v194, v212, v36, 0\n\t"
    "v_dot2_f32_bf16 v195, v212, v212, 0\n\t"
    "v_dot2_f32_bf16 v186, v197, v36, v186\n\t"
    "v_dot2_f32_bf16 v187, v197, v197, v187\n\t"
    "v_dot2_f32_bf16 v188, v201, v36, v188\n\t"
    "v_dot2_f32_bf16 v189, v201, v201, v189\n\t"
    "v_dot2_f32_bf16 v190, v205, v36, v190\n\t"
    "v_dot2_f32_bf16 v191, v205, v205, v191\n\t"
    "v_dot2_f32_bf16 v192, v209, v36, v192\n\t"
    "v_dot2_f32_bf16 v193, v209, v209, v193\n\t"
    "v_dot2_f32_bf16 v194, v213, v36, v194\n\t"
    "v_dot2_f32_bf16 v195, v213, v213, v195\n\t"
    "v_dot2_f32_bf16 v186, v198, v36, v186\n\t"
    "v_dot2_f32_bf16 v187, v198, v198, v187\n\t"
    "v_dot2_f32_bf16 v188, v202, v36, v188\n\t"
    "v_dot2_f32_bf16 v189, v202, v202, v189\n\t"
    "v_dot2_f32_bf16 v190, v206, v36, v190\n\t"
    "v_dot2_f32_bf16 v191, v206, v206, v191\n\t"
    "v_dot2_f32_bf16 v192, v210, v36, v192\n\t"
    "v_dot2_f32_bf16 v193, v210, v210, v193\n\t"
    "v_dot2_f32_bf16 v194, v214, v36, v194\n\t"
    "v_dot2_f32_bf16 v195, v214, v214, v195\n\t"
    "v_dot2_f32_bf16 v186, v199, v36, v186\n\t"
    "v_dot2_f32_bf16 v187, v199, v199, v187\n\t"
    "v_dot2_f32_bf16 v188, v203, v36, v188\n\t"
    "v_dot2_f32_bf16 v189, v203, v203, v189\n\t"
    "v_dot2_f32_bf16 v190, v207, v36, v190\n\t"
    "v_dot2_f32_bf16 v191, v207, v207, v191\n\t"
    "v_dot2_f32_bf16 v192, v211, v36, v192\n\t"
    "v_dot2_f32_bf16 v193, v211, v211, v193\n\t"
    "v_dot2_f32_bf16 v194, v215, v36, v194\n\t"
    "v_dot2_f32_bf16 v195, v215, v215, v195\n\t"
    "s_add_u32 m0, s48, 20480\n\t"
    "s_mov_b32 exec_lo, s62\n\t"
    "buffer_load_dwordx4 v2, s[8:11], s59 offen offset:0 lds\n\t"
    "s_mov_b32 exec_lo, -1\n\t"
    "buffer_load_dwordx4 v3, s[8:11], s59 offen offset:1024 lds\n\t"
    "buffer_load_dwordx4 v4, s[8:11], s59 offen offset:2048 lds\n\t"
    "buffer_load_dwordx4 v5, s[8:11], s59 offen offset:3072 lds\n\t"
    "s_add_u32 m0, s48, 24576\n\t"
    "s_nop 0\n\t"
    "buffer_load_dwordx4 v6, s[8:11], s59 offen offset:0 lds\n\t"
    "s_waitcnt vmcnt(14)\n\t"
    "s_barrier\n\t"
    "ds_read_b64_tr_b16 v[98:99], v33 offset:1024\n\t"
    "ds_read_b64_tr_b16 v[100:101], v33 offset:1536\n\t"
    "ds_read_b128 v[54:57], v29 offset:0\n\t"
    "ds_read_b128 v[58:61], v29 offset:4096\n\t"
    "ds_read_b64_tr_b16 v[102:103], v33 offset:2048\n\t"
    "ds_read_b64_tr_b16 v[104:105], v33 offset:2560\n\t"
    "ds_read_b128 v[62:65], v29 offset:8192\n\t"
    "ds_read_b64_tr_b16 v[94:95], v33 offset:0\n\t"
    "ds_read_b64_tr_b16 v[96:97], v33 offset:512\n\t"
    "ds_read_b128 v[66:69], v29 offset:12288\n\t"
    "ds_read_b128 v[70:73], v29 offset:16384\n\t"
    "s_waitcnt lgkmcnt(0)\n\t"
    "v_mfma_f32_16x16x32_bf16 v[134:137], v[98:101], v[54:57], 0\n\t"
    "ds_read_b64_tr_b16 v[110:111], v33 offset:13312\n\t"
    "s_add_u32 m0, s49, 49152\n\t"
    "s_nop 0\n\t"
    "buffer_load_dwordx4 v13, s[12:15], s52 offen offset:0 lds\n\t"
    "v_mfma_f32_16x16x32_bf16 v[138:141], v[98:101], v[58:61], 0\n\t"
    "ds_read_b64_tr_b16 v[112:113], v33 offset:13824\n\t"
    "v_mfma_f32_16x16x32_bf16 v[154:157], v[54:57], v[102:105], 0\n\t"
    "ds_read_b128 v[74:77], v30 offset:0\n\t"
    "buffer_load_dwordx4 v13, s[12:15], s52 offen offset:1024 lds\n\t"
    "v_mfma_f32_16x16x32_bf16 v[158:161], v[58:61], v[102:105], 0\n\t"
    "ds_read_b128 v[78:81], v30 offset:4096\n\t"
    "v_mfma_f32_16x16x32_bf16 v[142:145], v[98:101], v[62:65], 0\n\t"
    "ds_read_b64_tr_b16 v[114:115], v33 offset:14336\n\t"
    "v_mfma_f32_16x16x32_bf16 v[162:165], v[62:65], v[102:105], 0\n\t"
    "ds_read_b64_tr_b16 v[116:117], v33 offset:14848\n\t"
    "buffer_load_dwordx4 v13, s[12:15], s52 offen offset:2048 lds\n\t"
    "v_mfma_f32_16x16x32_bf16 v[174:177], v[94:97], v[36:39], 0\n\t"
    "ds_read_b128 v[82:85], v30 offset:8192\n\t"
    "v_mfma_f32_16x16x32_bf16 v[118:121], v[94:97], v[58:61], 0\n\t"
    "ds_read_b64_tr_b16 v[106:107], v33 offset:12288\n\t"
    "s_add_u32 m0, s49, 52224\n\t"
    "s_nop 0\n\t"
    "buffer_load_dwordx4 v14, s[12:15], s52 offen offset:0 lds\n\t"
    "v_mfma_f32_16x16x32_bf16 v[122:125], v[94:97], v[62:65], 0\n\t"
    "ds_read_b64_tr_b16 v[108:109], v33 offset:12800\n\t"
    "v_mfma_f32_16x16x32_bf16 v[146:149], v[98:101], v[66:69], 0\n\t"
    "ds_read_b128 v[86:89], v30 offset:12288\n\t"
    "v_mfma_f32_16x16x32_bf16 v[166:169], v[66:69], v[102:105], 0\n\t"
    "ds_read_b128 v[90:93], v30 offset:16384\n\t"
    "buffer_load_dwordx4 v14, s[12:15], s52 offen offset:1024 lds\n\t"
    "v_mfma_f32_16x16x32_bf16 v[126:129], v[94:97], v[66:69], 0\n\t"
    "v_mfma_f32_16x16x32_bf16 v[150:153], v[98:101], v[70:73], 0\n\t"
    "buffer_load_dwordx4 v14, s[12:15], s52 offen offset:2048 lds\n\t"
    "v_mfma_f32_16x16x32_bf16 v[170:173], v[70:73], v[102:105], 0\n\t"
    "v_mfma_f32_16x16x32_bf16 v[130:133], v[94:97], v[70:73], 0\n\t"
    "s_waitcnt lgkmcnt(0)\n\t"
    "s_waitcnt vmcnt(11)\n\t"
    "s_barrier\n\t"
    "v_mfma_f32_16x16x32_bf16 v[134:137], v[110:113], v[74:77], v[134:137]\n\t"
    "ds_read_b64_tr_b16 v[98:99], v33 offset:25600\n\t"
    "s_add_u32 m0, s48, 40960\n\t"
    "s_mov_b32 exec_lo, s62\n\t"
    "buffer_load_dwordx4 v2, s[8:11], s60 offen offset:0 lds\n\t"
    "s_mov_b32 exec_lo, -1\n\t"
    "v_mfma_f32_16x16x32_bf16 v[138:141], v[110:113], v[78:81], v[138:141]\n\t"
    "ds_read_b64_tr_b16 v[100:101], v33 offset:26112\n\t"
    "v_mfma_f32_16x16x32_bf16 v[154:157], v[74:77], v[114:117], v[154:157]\n\t"
    "ds_read_b128 v[54:57], v31 offset:0\n\t"
    "v_mfma_f32_16x16x32_bf16 v[158:161], v[78:81], v[114:117], v[158:161]\n\t"
    "ds_read_b128 v[58:61], v31 offset:4096\n\t"
    "buffer_load_dwordx4 v3, s[8:11], s60 offen offset:1024 lds\n\t"
    "v_mfma_f32_16x16x32_bf16 v[142:145], v[110:113], v[82:85], v[142:145]\n\t"
    "ds_read_b64_tr_b16 v[102:103], v33 offset:26624\n\t"
    "v_mfma_f32_16x16x32_bf16 v[162:165], v[82:85], v[114:117], v[162:165]\n\t"
    "ds_read_b64_tr_b16 v[104:105], v33 offset:27136\n\t"
    "v_mfma_f32_16x16x32_bf16 v[174:177], v[106:109], v[36:39], v[174:177]\n\t"
    "ds_read_b128 v[62:65], v31 offset:8192\n\t"
    "buffer_load_dwordx4 v4, s[8:11], s60 offen offset:2048 lds\n\t"
    "v_mfma_f32_16x16x32_bf16 v[118:121], v[106:109], v[78:81], v[118:121]\n\t"
    "ds_read_b64_tr_b16 v[94:95], v33 offset:24576\n\t"
    "v_mfma_f32_16x16x32_bf16 v[122:125], v[106:109], v[82:85], v[122:125]\n\t"
    "ds_read_b64_tr_b16 v[96:97], v33 offset:25088\n\t"
    "v_mfma_f32_16x16x32_bf16 v[146:149], v[110:113], v[86:89], v[146:149]\n\t"
    "ds_read_b128 v[66:69], v31 offset:12288\n\t"
    "buffer_load_dwordx4 v5, s[8:11], s60 offen offset:3072 lds\n\t"
    "v_mfma_f32_16x16x32_bf16 v[166:169], v[86:89], v[114:117], v[166:169]\n\t"
    "ds_read_b128 v[70:73], v31 offset:16384\n\t"
    "v_mfma_f32_16x16x32_bf16 v[126:129], v[106:109], v[86:89], v[126:129]\n\t"
    "v_mfma_f32_16x16x32_bf16 v[150:153], v[110:113], v[90:93], v[150:153]\n\t"
    "s_add_u32 m0, s48, 45056\n\t"
    "s_nop 0\n\t"
    "buffer_load_dwordx4 v6, s[8:11], s60 offen offset:0 lds\n\t"
    "v_mfma_f32_16x16x32_bf16 v[170:173], v[90:93], v[114:117], v[170:173]\n\t"
    "v_mfma_f32_16x16x32_bf16 v[130:133], v[106:109], v[90:93], v[130:133]\n\t"
    "s_waitcnt lgkmcnt(0)\n\t"
    "v_mfma_f32_16x16x32_bf16 v[134:137], v[98:101], v[54:57], v[134:137]\n\t"
    "ds_read_b64_tr_b16 v[110:111], v33 offset:37888\n\t"
    "s_add_u32 m0, s49, 0\n\t"
    "s_nop 0\n\t"
    "buffer_load_dwordx4 v13, s[12:15], s53 offen offset:0 lds\n\t"
    "v_mfma_f32_16x16x32_bf16 v[138:141], v[98:101], v[58:61], v[138:141]\n\t"
    "ds_read_b64_tr_b16 v[112:113], v33 offset:38400\n\t"
    "v_mfma_f32_16x16x32_bf16 v[154:157], v[54:57], v[102:105], v[154:157]\n\t"
    "ds_read_b128 v[74:77], v32 offset:0\n\t"
    "buffer_load_dwordx4 v13, s[12:15], s53 offen offset:1024 lds\n\t"
    "v_mfma_f32_16x16x32_bf16 v[158:161], v[58:61], v[102:105], v[158:161]\n\t"
    "ds_read_b128 v[78:81], v32 offset:4096\n\t"
    "v_mfma_f32_16x16x32_bf16 v[142:145], v[98:101], v[62:65], v[142:145]\n\t"
    "ds_read_b64_tr_b16 v[114:115], v33 offset:38912\n\t"
    "v_mfma_f32_16x16x32_bf16 v[162:165], v[62:65], v[102:105], v[162:165]\n\t"
    "ds_read_b64_tr_b16 v[116:117], v33 offset:39424\n\t"
    "buffer_load_dwordx4 v13, s[12:15], s53 offen offset:2048 lds\n\t"
    "v_mfma_f32_16x16x32_bf16 v[174:177], v[94:97], v[36:39], v[174:177]\n\t"
    "ds_read_b128 v[82:85], v32 offset:8192\n\t"
    "v_mfma_f32_16x16x32_bf16 v[118:121], v[94:97], v[58:61], v[118:121]\n\t"
    "ds_read_b64_tr_b16 v[106:107], v33 offset:36864\n\t"
    "s_add_u32 m0, s49, 3072\n\t"
    "s_nop 0\n\t"
    "buffer_load_dwordx4 v14, s[12:15], s53 offen offset:0 lds\n\t"
    "v_mfma_f32_16x16x32_bf16 v[122:125], v[94:97], v[62:65], v[122:125]\n\t"
    "ds_read_b64_tr_b16 v[108:109], v33 offset:37376\n\t"
    "v_mfma_f32_16x16x32_bf16 v[146:149], v[98:101], v[66:69], v[146:149]\n\t"
    "ds_read_b128 v[86:89], v32 offset:12288\n\t"
    "v_mfma_f32_16x16x32_bf16 v[166:169], v[66:69], v[102:105], v[166:169]\n\t"
    "s_waitcnt vmcnt(15)\n\t"
    "ds_read_b128 v[196:199], v35 offset:20480\n\t"
    "ds_read_b128 v[90:93], v32 offset:16384\n\t"
    "buffer_load_dwordx4 v14, s[12:15], s53 offen offset:1024 lds\n\t"
    "v_mfma_f32_16x16x32_bf16 v[126:129], v[94:97], v[66:69], v[126:129]\n\t"
    "ds_read_b128 v[200:203], v35 offset:21504\n\t"
    "v_mfma_f32_16x16x32_bf16 v[150:153], v[98:101], v[70:73], v[150:153]\n\t"
    "ds_read_b128 v[204:207], v35 offset:22528\n\t"
    "buffer_load_dwordx4 v14, s[12:15], s53 offen offset:2048 lds\n\t"
    "v_mfma_f32_16x16x32_bf16 v[170:173], v[70:73], v[102:105], v[170:173]\n\t"
    "ds_read_b128 v[208:211], v35 offset:23552\n\t"
    "v_mfma_f32_16x16x32_bf16 v[130:133], v[94:97], v[70:73], v[130:133]\n\t"
    "ds_read_b128 v[212:215], v35 offset:24576\n\t"
    "s_waitcnt lgkmcnt(5)\n\t"
    "v_dot2_f32_bf16 v186, v196, v36, v186\n\t"
    "v_dot2_f32_bf16 v187, v196, v196, v187\n\t"
    "s_waitcnt lgkmcnt(3)\n\t"
    "v_dot2_f32_bf16 v188, v200, v36, v188\n\t"
    "v_dot2_f32_bf16 v189, v200, v200, v189\n\t"
    "s_waitcnt lgkmcnt(2)\n\t"
    "v_dot2_f32_bf16 v190, v204, v36, v190\n\t"
    "v_dot2_f32_bf16 v191, v204, v204, v191\n\t"
    "s_waitcnt lgkmcnt(1)\n\t"
    "v_dot2_f32_bf16 v192, v208, v36, v192\n\t"
    "v_dot2_f32_bf16 v193, v208, v208, v193\n\t"
    "s_waitcnt lgkmcnt(0)\n\t"
    "v_dot2_f32_bf16 v194, v212, v36, v194\n\t"
    "v_dot2_f32_bf16 v195, v212, v212, v195\n\t"
    "v_dot2_f32_bf16 v186, v197, v36, v186\n\t"
    "v_dot2_f32_bf16 v187, v197, v197, v187\n\t"
    "v_dot2_f32_bf16 v188, v201, v36, v188\n\t"
    "v_dot2_f32_bf16 v189, v201, v201, v189\n\t"
    "v_dot2_f32_bf16 v190, v205, v36, v190\n\t"
    "v_dot2_f32_bf16 v191, v205, v205, v191\n\t"
    "v_dot2_f32_bf16 v192, v209, v36, v192\n\t"
    "v_dot2_f32_bf16 v193, v209, v209, v193\n\t"
    "v_dot2_f32_bf16 v194, v213, v36, v194\n\t"
    "v_dot2_f32_bf16 v195, v213, v213, v195\n\t"
    "v_dot2_f32_bf16 v186, v198, v36, v186\n\t"
    "v_dot2_f32_bf16 v187, v198, v198, v187\n\t"
    "v_dot2_f32_bf16 v188, v202, v36, v188\n\t"
    "v_dot2_f32_bf16 v189, v202, v202, v189\n\t"
    "v_dot2_f32_bf16 v190, v206, v36, v190\n\t"
    "v_dot2_f32_bf16 v191, v206, v206, v191\n\t"
    "v_dot2_f32_bf16 v192, v210, v36, v192\n\t"
    "v_dot2_f32_bf16 v193, v210, v210, v193\n\t"
    "v_dot2_f32_bf16 v194, v214, v36, v194\n\t"
    "v_dot2_f32_bf16 v195, v214, v214, v195\n\t"
    "v_dot2_f32_bf16 v186, v199, v36, v186\n\t"
    "v_dot2_f32_bf16 v187, v199, v199, v187\n\t"
    "v_dot2_f32_bf16 v188, v203, v36, v188\n\t"
    "v_dot2_f32_bf16 v189, v203, v203, v189\n\t"
    "v_dot2_f32_bf16 v190, v207, v36, v190\n\t"
    "v_dot2_f32_bf16 v191, v207, v207, v191\n\t"
    "v_dot2_f32_bf16 v192, v211, v36, v192\n\t"
    "v_dot2_f32_bf16 v193, v211, v211, v193\n\t"
    "v_dot2_f32_bf16 v194, v215, v36, v194\n\t"
    "v_dot2_f32_bf16 v195, v215, v215, v195\n\t"
    "s_waitcnt vmcnt(11)\n\t"
    "s_barrier\n\t"
    "v_mfma_f32_16x16x32_bf16 v[134:137], v[110:113], v[74:77], v[134:137]\n\t"
    "ds_read_b64_tr_b16 v[98:99], v34 offset:1024\n\t"
    "s_add_u32 m0, s49, 24576\n\t"
    "s_nop 0\n\t"
    "buffer_load_dwordx4 v13, s[12:15], s54 offen offset:0 lds\n\t"
    "v_mfma_f32_16x16x32_bf16 v[138:141], v[110:113], v[78:81], v[138:141]\n\t"
    "ds_read_b64_tr_b16 v[100:101], v34 offset:1536\n\t"
    "v_and_b32 v7, 1, v1\n\t"
    "v_mfma_f32_16x16x32_bf16 v[154:157], v[74:77], v[114:117], v[154:157]\n\t"
    "ds_read_b128 v[54:57], v29 offset:20480\n\t"
    "v_cmp_eq_u32 s[66:67], 1, v7\n\t"
    "v_lshlrev_b32 v40, 4, v7\n\t"
    "v_mfma_f32_16x16x32_bf16 v[158:161], v[78:81], v[114:117], v[158:161]\n\t"
    "ds_read_b128 v[58:61], v29 offset:24576\n\t"
    "buffer_load_dwordx4 v13, s[12:15], s54 offen offset:1024 lds\n\t"
    "v_mfma_f32_16x16x32_bf16 v[142:145], v[110:113], v[82:85], v[142:145]\n\t"
    "ds_read_b64_tr_b16 v[102:103], v34 offset:2048\n\t"
    "v_lshl_add_u32 v7, v12, 2, v7\n\t"
    "v_mfma_f32_16x16x32_bf16 v[162:165], v[82:85], v[114:117], v[162:165]\n\t"
    "ds_read_b64_tr_b16 v[104:105], v34 offset:2560\n\t"
    "v_cmp_le_u32 s[68:69], v7, v11\n\t"
    "v_mfma_f32_16x16x32_bf16 v[174:177], v[106:109], v[36:39], v[174:177]\n\t"
    "ds_read_b128 v[62:65], v29 offset:28672\n\t"
    "buffer_load_dwordx4 v13, s[12:15], s54 offen offset:2048 lds\n\t"
    "v_mfma_f32_16x16x32_bf16 v[118:121], v[106:109], v[78:81], v[118:121]\n\t"
    "ds_read_b64_tr_b16 v[94:95], v34 offset:0\n\t"
    "v_add_u32 v7, 2, v7\n\t"
    "v_cmp_le_u32 s[70:71], v7, v11\n\t"
    "v_mfma_f32_16x16x32_bf16 v[122:125], v[106:109], v[82:85], v[122:125]\n\t"
    "ds_read_b64_tr_b16 v[96:97], v34 offset:512\n\t"
    "s_cmp_eq_u32 s47, 0\n\t"
    "s_cselect_b64 s[72:73], -1, 0\n\t"
    "v_mfma_f32_16x16x32_bf16 v[146:149], v[110:113], v[86:89], v[146:149]\n\t"
    "ds_read_b128 v[66:69], v29 offset:32768\n\t"
    "s_add_u32 m0, s49, 27648\n\t"
    "s_nop 0\n\t"
    "buffer_load_dwordx4 v14, s[12:15], s54 offen offset:0 lds\n\t"
    "v_mfma_f32_16x16x32_bf16 v[166:169], v[86:89], v[114:117], v[166:169]\n\t"
    "ds_read_b128 v[70:73], v29 offset:36864\n\t"
    "s_lshl_b32 s63, s33, 13\n\t"
    "v_lshrrev_b32 v7, 3, v1\n\t"
    "v_mfma_f32_16x16x32_bf16 v[126:129], v[106:109], v[86:89], v[126:129]\n\t"
    "v_and_b32 v8, 2, v7\n\t"
    "v_lshlrev_b32 v8, 1, v8\n\t"
    "v_mfma_f32_16x16x32_bf16 v[150:153], v[110:113], v[90:93], v[150:153]\n\t"
    "buffer_load_dwordx4 v14, s[12:15], s54 offen offset:1024 lds\n\t"
    "v_mfma_f32_16x16x32_bf16 v[170:173], v[90:93], v[114:117], v[170:173]\n\t"
    "v_and_b32 v9, 7, v1\n\t"
    "v_xor_b32 v8, v8, v9\n\t"
    "v_lshlrev_b32 v7, 10, v7\n\t"
    "v_mfma_f32_16x16x32_bf16 v[130:133], v[106:109], v[90:93], v[130:133]\n\t"
    "v_lshl_add_u32 v41, v8, 4, v7\n\t"
    "s_lshl_b32 s74, s33, 17\n\t"
    "s_waitcnt lgkmcnt(0)\n\t"
    "v_mfma_f32_16x16x32_bf16 v[134:137], v[98:101], v[54:57], v[134:137]\n\t"
    "ds_read_b64_tr_b16 v[110:111], v34 offset:13312\n\t"
    "buffer_load_dwordx4 v14, s[12:15], s54 offen offset:2048 lds\n\t"
    "v_mfma_f32_16x16x32_bf16 v[138:141], v[98:101], v[58:61], v[138:141]\n\t"
    "ds_read_b64_tr_b16 v[112:113], v34 offset:13824\n\t"
    "s_lshl_b32 s39, s65, 7\n\t"
    "s_add_u32 s74, s74, s39\n\t"
    "s_and_b32 s39, s34, 1\n\t"
    "v_mfma_f32_16x16x32_bf16 v[154:157], v[54:57], v[102:105], v[154:157]\n\t"
    "ds_read_b128 v[74:77], v30 offset:20480\n\t"
    "s_mul_i32 s39, s39, 7\n\t"
    "s_mul_i32 s40, s35, 3\n\t"
    "s_add_u32 s78, s39, s40\n\t"
    "v_mfma_f32_16x16x32_bf16 v[158:161], v[58:61], v[102:105], v[158:161]\n\t"
    "ds_read_b128 v[78:81], v30 offset:24576\n\t"
    "s_add_u32 m0, s48, 61440\n\t"
    "s_mov_b32 exec_lo, s62\n\t"
    "buffer_load_dwordx4 v2, s[8:11], s61 offen offset:0 lds\n\t"
    "s_mov_b32 exec_lo, -1\n\t"
    "v_mfma_f32_16x16x32_bf16 v[142:145], v[98:101], v[62:65], v[142:145]\n\t"
    "ds_read_b64_tr_b16 v[114:115], v34 offset:14336\n\t"
    "s_lshr_b32 s40, s36, 3\n\t"
    "s_lshl2_add_u32 s40, s35, s40\n\t"
    "v_mfma_f32_16x16x32_bf16 v[162:165], v[62:65], v[102:105], v[162:165]\n\t"
    "ds_read_b64_tr_b16 v[116:117], v34 offset:14848\n\t"
    "s_and_b32 s41, s36, 3\n\t"
    "s_lshl2_add_u32 s41, s35, s41\n\t"
    "s_cmp_lt_u32 s36, 24\n\t"
    "s_cselect_b32 s40, s40, 3\n\t"
    "s_cselect_b32 s75, s65, s41\n\t"
    "v_mfma_f32_16x16x32_bf16 v[174:177], v[94:97], v[36:39], v[174:177]\n\t"
    "ds_read_b128 v[82:85], v30 offset:28672\n\t"
    "buffer_load_dwordx4 v3, s[8:11], s61 offen offset:1024 lds\n\t"
    "v_mfma_f32_16x16x32_bf16 v[118:121], v[94:97], v[58:61], v[118:121]\n\t"
    "ds_read_b64_tr_b16 v[106:107], v34 offset:12288\n\t"
    "s_add_u32 s76, s39, s40\n\t"
    "v_mfma_f32_16x16x32_bf16 v[122:125], v[94:97], v[62:65], v[122:125]\n\t"
    "ds_read_b64_tr_b16 v[108:109], v34 offset:12800\n\t"
    "s_lshr_b32 s39, s34, 1\n\t"
    "s_lshl3_add_u32 s77, s39, s75\n\t"
    "v_mfma_f32_16x16x32_bf16 v[146:149], v[98:101], v[66:69], v[146:149]\n\t"
    "ds_read_b128 v[86:89], v30 offset:32768\n\t"
    "buffer_load_dwordx4 v4, s[8:11], s61 offen offset:2048 lds\n\t"
    "v_mfma_f32_16x16x32_bf16 v[166:169], v[66:69], v[102:105], v[166:169]\n\t"
    "s_waitcnt vmcnt(15)\n\t"
    "ds_read_b128 v[196:199], v35 offset:40960\n\t"
    "ds_read_b128 v[90:93], v30 offset:36864\n\t"
    "v_lshrrev_b32 v7, 2, v1\n\t"
    "v_mfma_f32_16x16x32_bf16 v[126:129], v[94:97], v[66:69], v[126:129]\n\t"
    "ds_read_b128 v[200:203], v35 offset:41984\n\t"
    "v_mul_u32_u24 v7, 3584, v7\n\t"
    "v_mfma_f32_16x16x32_bf16 v[150:153], v[98:101], v[70:73], v[150:153]\n\t"
    "ds_read_b128 v[204:207], v35 offset:43008\n\t"
    "buffer_load_dwordx4 v5, s[8:11], s61 offen offset:3072 lds\n\t"
    "v_mfma_f32_16x16x32_bf16 v[170:173], v[70:73], v[102:105], v[170:173]\n\t"
    "ds_read_b128 v[208:211], v35 offset:44032\n\t"
    "v_and_b32 v8, 3, v1\n\t"
    "v_mfma_f32_16x16x32_bf16 v[130:133], v[94:97], v[70:73], v[130:133]\n\t"
    "ds_read_b128 v[212:215], v35 offset:45056\n\t"
    "v_lshl_add_u32 v42, v8, 4, v7\n\t"
    "s_lshl2_add_u32 s40, s76, s33\n\t"
    "s_waitcnt lgkmcnt(5)\n\t"
    "v_dot2_f32_bf16 v186, v196, v36, v186\n\t"
    "v_dot2_f32_bf16 v187, v196, v196, v187\n\t"
    "s_waitcnt lgkmcnt(3)\n\t"
    "v_dot2_f32_bf16 v188, v200, v36, v188\n\t"
    "v_dot2_f32_bf16 v189, v200, v200, v189\n\t"
    "s_waitcnt lgkmcnt(2)\n\t"
    "v_dot2_f32_bf16 v190, v204, v36, v190\n\t"
    "v_dot2_f32_bf16 v191, v204, v204, v191\n\t"
    "s_waitcnt lgkmcnt(1)\n\t"
    "v_dot2_f32_bf16 v192, v208, v36, v192\n\t"
    "v_dot2_f32_bf16 v193, v208, v208, v193\n\t"
    "s_waitcnt lgkmcnt(0)\n\t"
    "v_dot2_f32_bf16 v194, v212, v36, v194\n\t"
    "v_dot2_f32_bf16 v195, v212, v212, v195\n\t"
    "v_dot2_f32_bf16 v186, v197, v36, v186\n\t"
    "v_dot2_f32_bf16 v187, v197, v197, v187\n\t"
    "v_dot2_f32_bf16 v188, v201, v36, v188\n\t"
    "v_dot2_f32_bf16 v189, v201, v201, v189\n\t"
    "v_dot2_f32_bf16 v190, v205, v36, v190\n\t"
    "v_dot2_f32_bf16 v191, v205, v205, v191\n\t"
    "v_dot2_f32_bf16 v192, v209, v36, v192\n\t"
    "v_dot2_f32_bf16 v193, v209, v209, v193\n\t"
    "v_dot2_f32_bf16 v194, v213, v36, v194\n\t"
    "v_dot2_f32_bf16 v195, v213, v213, v195\n\t"
    "v_dot2_f32_bf16 v186, v198, v36, v186\n\t"
    "v_dot2_f32_bf16 v187, v198, v198, v187\n\t"
    "v_dot2_f32_bf16 v188, v202, v36, v188\n\t"
    "v_dot2_f32_bf16 v189, v202, v202, v189\n\t"
    "v_dot2_f32_bf16 v190, v206, v36, v190\n\t"
    "v_dot2_f32_bf16 v191, v206, v206, v191\n\t"
    "v_dot2_f32_bf16 v192, v210, v36, v192\n\t"
    "v_dot2_f32_bf16 v193, v210, v210, v193\n\t"
    "v_dot2_f32_bf16 v194, v214, v36, v194\n\t"
    "v_dot2_f32_bf16 v195, v214, v214, v195\n\t"
    "v_dot2_f32_bf16 v186, v199, v36, v186\n\t"
    "v_dot2_f32_bf16 v187, v199, v199, v187\n\t"
    "v_dot2_f32_bf16 v188, v203, v36, v188\n\t"
    "v_dot2_f32_bf16 v189, v203, v203, v189\n\t"
    "v_dot2_f32_bf16 v190, v207, v36, v190\n\t"
    "v_dot2_f32_bf16 v191, v207, v207, v191\n\t"
    "v_dot2_f32_bf16 v192, v211, v36, v192\n\t"
    "v_dot2_f32_bf16 v193, v211, v211, v193\n\t"
    "v_dot2_f32_bf16 v194, v215, v36, v194\n\t"
    "v_dot2_f32_bf16 v195, v215, v215, v195\n\t"
    "s_waitcnt vmcnt(10)\n\t"
    "s_barrier\n\t"
    "v_mfma_f32_16x16x32_bf16 v[134:137], v[110:113], v[74:77], v[134:137]\n\t"
    "ds_read_b64_tr_b16 v[98:99], v33 offset:1024\n\t"
    "s_add_u32 m0, s48, 65536\n\t"
    "s_nop 0\n\t"
    "buffer_load_dwordx4 v6, s[8:11], s61 offen offset:0 lds\n\t"
    "v_mfma_f32_16x16x32_bf16 v[138:141], v[110:113], v[78:81], v[138:141]\n\t"
    "ds_read_b64_tr_b16 v[100:101], v33 offset:1536\n\t"
    "s_lshl_b32 s40, s40, 6\n\t"
    "s_add_u32 s39, s65, 0\n\t"
    "v_mfma_f32_16x16x32_bf16 v[154:157], v[74:77], v[114:117], v[154:157]\n\t"
    "ds_read_b128 v[54:57], v31 offset:20480\n\t"
    "s_and_b32 s39, s39, 7\n\t"
    "s_mul_i32 s39, s39, 229376\n\t"
    "s_add_u32 s79, s39, s40\n\t"
    "v_mfma_f32_16x16x32_bf16 v[158:161], v[78:81], v[114:117], v[158:161]\n\t"
    "ds_read_b128 v[58:61], v31 offset:24576\n\t"
    "s_add_u32 m0, s49, 49152\n\t"
    "s_nop 0\n\t"
    "buffer_load_dwordx4 v13, s[12:15], s55 offen offset:0 lds\n\t"
    "v_mfma_f32_16x16x32_bf16 v[142:145], v[110:113], v[82:85], v[142:145]\n\t"
    "ds_read_b64_tr_b16 v[102:103], v33 offset:2048\n\t"
    "s_add_u32 s39, s65, 1\n\t"
    "s_and_b32 s39, s39, 7\n\t"
    "s_mul_i32 s39, s39, 229376\n\t"
    "v_mfma_f32_16x16x32_bf16 v[162:165], v[82:85], v[114:117], v[162:165]\n\t"
    "ds_read_b64_tr_b16 v[104:105], v33 offset:2560\n\t"
    "s_add_u32 s80, s39, s40\n\t"
    "s_add_u32 s39, s65, 2\n\t"
    "v_mfma_f32_16x16x32_bf16 v[174:177], v[106:109], v[36:39], v[174:177]\n\t"
    "ds_read_b128 v[62:65], v31 offset:28672\n\t"
    "buffer_load_dwordx4 v13, s[12:15], s55 offen offset:1024 lds\n\t"
    "v_mfma_f32_16x16x32_bf16 v[118:121], v[106:109], v[78:81], v[118:121]\n\t"
    "ds_read_b64_tr_b16 v[94:95], v33 offset:0\n\t"
    "s_and_b32 s39, s39, 7\n\t"
    "s_mul_i32 s39, s39, 229376\n\t"
    "s_add_u32 s81, s39, s40\n\t"
    "v_mfma_f32_16x16x32_bf16 v[122:125], v[106:109], v[82:85], v[122:125]\n\t"
    "ds_read_b64_tr_b16 v[96:97], v33 offset:512\n\t"
    "s_add_u32 s39, s65, 3\n\t"
    "s_and_b32 s39, s39, 7\n\t"
    "v_mfma_f32_16x16x32_bf16 v[146:149], v[110:113], v[86:89], v[146:149]\n\t"
    "ds_read_b128 v[66:69], v31 offset:32768\n\t"
    "s_mul_i32 s39, s39, 229376\n\t"
    "s_add_u32 s82, s39, s40\n\t"
    "s_add_u32 s39, s65, 4\n\t"
    "v_mfma_f32_16x16x32_bf16 v[166:169], v[86:89], v[114:117], v[166:169]\n\t"
    "ds_read_b128 v[70:73], v31 offset:36864\n\t"
    "buffer_load_dwordx4 v13, s[12:15], s55 offen offset:2048 lds\n\t"
    "v_mfma_f32_16x16x32_bf16 v[126:129], v[106:109], v[86:89], v[126:129]\n\t"
    "s_and_b32 s39, s39, 7\n\t"
    "s_mul_i32 s39, s39, 229376\n\t"
    "s_add_u32 s83, s39, s40\n\t"
    "s_add_u32 s39, s65, 5\n\t"
    "s_and_b32 s39, s39, 7\n\t"
    "v_mfma_f32_16x16x32_bf16 v[150:153], v[110:113], v[90:93], v[150:153]\n\t"
    "s_mul_i32 s39, s39, 229376\n\t"
    "s_add_u32 s84, s39, s40\n\t"
    "s_add_u32 s39, s65, 6\n\t"
    "s_and_b32 s39, s39, 7\n\t"
    "v_mfma_f32_16x16x32_bf16 v[170:173], v[90:93], v[114:117], v[170:173]\n\t"
    "s_add_u32 m0, s49, 52224\n\t"
    "s_nop 0\n\t"
    "buffer_load_dwordx4 v14, s[12:15], s55 offen offset:0 lds\n\t"
    "v_mfma_f32_16x16x32_bf16 v[130:133], v[106:109], v[90:93], v[130:133]\n\t"
    "s_mul_i32 s39, s39, 229376\n\t"
    "s_add_u32 s85, s39, s40\n\t"
    "s_add_u32 s39, s65, 7\n\t"
    "s_and_b32 s39, s39, 7\n\t"
    "s_mul_i32 s39, s39, 229376\n\t"
    "s_waitcnt lgkmcnt(0)\n\t"
    "v_mfma_f32_16x16x32_bf16 v[134:137], v[98:101], v[54:57], v[134:137]\n\t"
    "ds_read_b64_tr_b16 v[110:111], v33 offset:13312\n\t"
    "s_add_u32 s86, s39, s40\n\t"
    "s_bitcmp1_b32 s33, 0\n\t"
    "s_cselect_b64 s[88:89], s[24:25], s[20:21]\n\t"
    "v_mfma_f32_16x16x32_bf16 v[138:141], v[98:101], v[58:61], v[138:141]\n\t"
    "ds_read_b64_tr_b16 v[112:113], v33 offset:13824\n\t"
    "s_lshl2_add_u32 s39, s43, s33\n\t"
    "s_lshl_b32 s40, s39, 9\n\t"
    "v_mfma_f32_16x16x32_bf16 v[154:157], v[54:57], v[102:105], v[154:157]\n\t"
    "ds_read_b128 v[74:77], v32 offset:20480\n\t"
    "buffer_load_dwordx4 v14, s[12:15], s55 offen offset:1024 lds\n\t"
    "v_mfma_f32_16x16x32_bf16 v[158:161], v[58:61], v[102:105], v[158:161]\n\t"
    "ds_read_b128 v[78:81], v32 offset:24576\n\t"
    "v_and_b32 v7, 3, v1\n\t"
    "v_lshlrev_b32 v7, 7, v7\n\t"
    "v_mfma_f32_16x16x32_bf16 v[142:145], v[98:101], v[62:65], v[142:145]\n\t"
    "ds_read_b64_tr_b16 v[114:115], v33 offset:14336\n\t"
    "v_add_u32 v43, s40, v7\n\t"
    "v_mfma_f32_16x16x32_bf16 v[162:165], v[62:65], v[102:105], v[162:165]\n\t"
    "ds_read_b64_tr_b16 v[116:117], v33 offset:14848\n\t"
    "buffer_load_dwordx4 v14, s[12:15], s55 offen offset:2048 lds\n\t"
    "v_mfma_f32_16x16x32_bf16 v[174:177], v[94:97], v[36:39], v[174:177]\n\t"
    "ds_read_b128 v[82:85], v32 offset:28672\n\t"
    "s_and_b32 s40, s39, 127\n\t"
    "s_mul_i32 s40, s40, 14336\n\t"
    "v_mfma_f32_16x16x32_bf16 v[118:121], v[94:97], v[58:61], v[118:121]\n\t"
    "ds_read_b64_tr_b16 v[106:107], v33 offset:12288\n\t"
    "v_min_u32 v7, 55, v1\n\t"
    "v_lshlrev_b32 v7, 8, v7\n\t"
    "v_mfma_f32_16x16x32_bf16 v[122:125], v[94:97], v[62:65], v[122:125]\n\t"
    "ds_read_b64_tr_b16 v[108:109], v33 offset:12800\n\t"
    "v_add_u32 v45, s40, v7\n\t"
    "v_mfma_f32_16x16x32_bf16 v[146:149], v[98:101], v[66:69], v[146:149]\n\t"
    "ds_read_b128 v[86:89], v32 offset:32768\n\t"
    "s_lshl2_add_u32 s39, s36, s33\n\t"
    "s_lshr_b32 s39, s39, 1\n\t"
    "v_mfma_f32_16x16x32_bf16 v[166:169], v[66:69], v[102:105], v[166:169]\n\t"
    "ds_read_b128 v[90:93], v32 offset:36864\n\t"
    "s_bfe_u32 s40, s43, 0x10002\n\t"
    "s_lshl_b32 s40, s40, 6\n\t"
    "s_add_u32 s39, s39, s40\n\t"
    "v_mfma_f32_16x16x32_bf16 v[126:129], v[94:97], v[66:69], v[126:129]\n\t"
    "s_mul_i32 s39, s39, 14336\n\t"
    "s_lshl_b32 s40, s78, 8\n\t"
    "s_add_u32 s39, s39, s40\n\t"
    "v_lshrrev_b32 v8, 3, v17\n\t"
    "v_add_u32 v29, 40960, v29\n\t"
    "v_mfma_f32_16x16x32_bf16 v[150:153], v[98:101], v[70:73], v[150:153]\n\t"
    "v_mul_u32_u24 v8, 3584, v8\n\t"
    "v_lshlrev_b32 v7, 7, v17\n\t"
    "v_add_u32 v30, 40960, v30\n\t"
    "v_mfma_f32_16x16x32_bf16 v[170:173], v[70:73], v[102:105], v[170:173]\n\t"
    "v_and_b32 v7, 0x380, v7\n\t"
    "v_add3_u32 v44, s39, v7, v8\n\t"
    "v_add_u32 v31, 40960, v31\n\t"
    "v_mfma_f32_16x16x32_bf16 v[130:133], v[94:97], v[70:73], v[130:133]\n\t"
    "v_bfe_u32 v7, v1, 2, 2\n\t"
    "v_lshl_add_u32 v8, v18, 2, v7\n\t"
    "v_lshlrev_b32 v8, 6, v8\n\t"
    "v_add_u32 v32, 40960, v32\n\t"
    "s_waitcnt lgkmcnt(0)\n\t"
    "s_waitcnt vmcnt(11)\n\t"
    "s_barrier\n\t"
    "v_mfma_f32_16x16x32_bf16 v[134:137], v[110:113], v[74:77], v[134:137]\n\t"
    "ds_read_b64_tr_b16 v[98:99], v33 offset:25600\n\t"
    "s_add_u32 m0, s49, 0\n\t"
    "s_nop 0\n\t"
    "buffer_load_dwordx4 v13, s[12:15], s56 offen offset:0 lds\n\t"
    "v_mfma_f32_16x16x32_bf16 v[138:141], v[110:113], v[78:81], v[138:141]\n\t"
    "ds_read_b64_tr_b16 v[100:101], v33 offset:26112\n\t"
    "v_and_b32 v9, 1, v12\n\t"
    "v_mfma_f32_16x16x32_bf16 v[154:157], v[74:77], v[114:117], v[154:157]\n\t"
    "ds_read_b128 v[54:57], v29 offset:0\n\t"
    "v_lshl_add_u32 v8, v9, 5, v8\n\t"
    "v_mfma_f32_16x16x32_bf16 v[158:161], v[78:81], v[114:117], v[158:161]\n\t"
    "ds_read_b128 v[58:61], v29 offset:4096\n\t"
    "buffer_load_dwordx4 v13, s[12:15], s56 offen offset:1024 lds\n\t"
    "v_mfma_f32_16x16x32_bf16 v[142:145], v[110:113], v[82:85], v[142:145]\n\t"
    "ds_read_b64_tr_b16 v[102:103], v33 offset:26624\n\t"
    "v_and_b32 v10, 3, v1\n\t"
    "v_lshl_add_u32 v8, v10, 3, v8\n\t"
    "v_mfma_f32_16x16x32_bf16 v[162:165], v[82:85], v[114:117], v[162:165]\n\t"
    "ds_read_b64_tr_b16 v[104:105], v33 offset:27136\n\t"
    "v_add_u32 v46, s63, v8\n\t"
    "v_mfma_f32_16x16x32_bf16 v[174:177], v[106:109], v[36:39], v[174:177]\n\t"
    "ds_read_b128 v[62:65], v29 offset:8192\n\t"
    "v_add_u32 v47, 65536, v46\n\t"
    "v_mfma_f32_16x16x32_bf16 v[118:121], v[106:109], v[78:81], v[118:121]\n\t"
    "ds_read_b64_tr_b16 v[94:95], v33 offset:24576\n\t"
    "buffer_load_dwordx4 v13, s[12:15], s56 offen offset:2048 lds\n\t"
    "v_mfma_f32_16x16x32_bf16 v[122:125], v[106:109], v[82:85], v[122:125]\n\t"
    "ds_read_b64_tr_b16 v[96:97], v33 offset:25088\n\t"
    "v_add_u32 v48, 131072, v46\n\t"
    "v_lshl_add_u32 v8, v18, 3, v7\n\t"
    "v_mfma_f32_16x16x32_bf16 v[146:149], v[110:113], v[86:89], v[146:149]\n\t"
    "ds_read_b128 v[66:69], v29 offset:12288\n\t"
    "v_lshlrev_b32 v8, 7, v8\n\t"
    "v_mfma_f32_16x16x32_bf16 v[166:169], v[86:89], v[114:117], v[166:169]\n\t"
    "ds_read_b128 v[70:73], v29 offset:16384\n\t"
    "v_lshl_add_u32 v8, v9, 5, v8\n\t"
    "v_mfma_f32_16x16x32_bf16 v[126:129], v[106:109], v[86:89], v[126:129]\n\t"
    "s_add_u32 m0, s49, 3072\n\t"
    "s_nop 0\n\t"
    "buffer_load_dwordx4 v14, s[12:15], s56 offen offset:0 lds\n\t"
    "v_mfma_f32_16x16x32_bf16 v[150:153], v[110:113], v[90:93], v[150:153]\n\t"
    "v_lshl_add_u32 v8, v10, 3, v8\n\t"
    "v_add_u32 v8, s63, v8\n\t"
    "v_add_u32 v8, 65536, v8\n\t"
    "v_mfma_f32_16x16x32_bf16 v[170:173], v[90:93], v[114:117], v[170:173]\n\t"
    "v_lshrrev_b32 v7, 1, v7\n\t"
    "v_xor_b32 v7, s33, v7\n\t"
    "v_mfma_f32_16x16x32_bf16 v[130:133], v[106:109], v[90:93], v[130:133]\n\t"
    "v_and_b32 v7, 1, v7\n\t"
    "v_lshl_add_u32 v49, v7, 6, v8\n\t"
    "s_waitcnt lgkmcnt(0)\n\t"
    "v_mfma_f32_16x16x32_bf16 v[134:137], v[98:101], v[54:57], v[134:137]\n\t"
    "ds_read_b64_tr_b16 v[110:111], v33 offset:37888\n\t"
    "buffer_load_dwordx4 v14, s[12:15], s56 offen offset:1024 lds\n\t"
    "v_mfma_f32_16x16x32_bf16 v[138:141], v[98:101], v[58:61], v[138:141]\n\t"
    "ds_read_b64_tr_b16 v[112:113], v33 offset:38400\n\t"
    "v_xor_b32 v50, 64, v49\n\t"
    "s_lshr_b32 s39, s36, 3\n\t"
    "v_mfma_f32_16x16x32_bf16 v[154:157], v[54:57], v[102:105], v[154:157]\n\t"
    "ds_read_b128 v[74:77], v30 offset:0\n\t"
    "s_lshl1_add_u32 s87, s39, s65\n\t"
    "s_and_b32 s87, s87, 7\n\t"
    "v_mfma_f32_16x16x32_bf16 v[158:161], v[58:61], v[102:105], v[158:161]\n\t"
    "ds_read_b128 v[78:81], v30 offset:4096\n\t"
    "v_add_u32 v51, s74, v41\n\t"
    "v_add_u32 v52, 7168, v51\n\t"
    "v_mfma_f32_16x16x32_bf16 v[142:145], v[98:101], v[62:65], v[142:145]\n\t"
    "ds_read_b64_tr_b16 v[114:115], v33 offset:38912\n\t"
    "buffer_load_dwordx4 v14, s[12:15], s56 offen offset:2048 lds\n\t"
    "v_mfma_f32_16x16x32_bf16 v[162:165], v[62:65], v[102:105], v[162:165]\n\t"
    "ds_read_b64_tr_b16 v[116:117], v33 offset:39424\n\t"
    "v_add_u32 v10, 61440, v35\n\t"
    "v_mfma_f32_16x16x32_bf16 v[174:177], v[94:97], v[36:39], v[174:177]\n\t"
    "ds_read_b128 v[82:85], v30 offset:8192\n\t"
    "v_mfma_f32_16x16x32_bf16 v[118:121], v[94:97], v[58:61], v[118:121]\n\t"
    "ds_read_b64_tr_b16 v[106:107], v33 offset:36864\n\t"
    "v_mfma_f32_16x16x32_bf16 v[122:125], v[94:97], v[62:65], v[122:125]\n\t"
    "ds_read_b64_tr_b16 v[108:109], v33 offset:37376\n\t"
    "v_mfma_f32_16x16x32_bf16 v[146:149], v[98:101], v[66:69], v[146:149]\n\t"
    "ds_read_b128 v[86:89], v30 offset:12288\n\t"
    "v_mfma_f32_16x16x32_bf16 v[166:169], v[66:69], v[102:105], v[166:169]\n\t"
    "s_waitcnt vmcnt(12)\n\t"
    "ds_read_b128 v[196:199], v10 offset:0\n\t"
    "ds_read_b128 v[90:93], v30 offset:16384\n\t"
    "v_mfma_f32_16x16x32_bf16 v[126:129], v[94:97], v[66:69], v[126:129]\n\t"
    "ds_read_b128 v[200:203], v10 offset:1024\n\t"
    "v_mfma_f32_16x16x32_bf16 v[150:153], v[98:101], v[70:73], v[150:153]\n\t"
    "ds_read_b128 v[204:207], v10 offset:2048\n\t"
    "v_mfma_f32_16x16x32_bf16 v[170:173], v[70:73], v[102:105], v[170:173]\n\t"
    "ds_read_b128 v[208:211], v10 offset:3072\n\t"
    "v_mfma_f32_16x16x32_bf16 v[130:133], v[94:97], v[70:73], v[130:133]\n\t"
    "ds_read_b128 v[212:215], v10 offset:4096\n\t"
    "global_load_dword v15, v44, s[88:89]\n\t"
    "s_waitcnt lgkmcnt(5)\n\t"
    "v_dot2_f32_bf16 v186, v196, v36, v186\n\t"
    "v_dot2_f32_bf16 v187, v196, v196, v187\n\t"
    "s_waitcnt lgkmcnt(3)\n\t"
    "v_dot2_f32_bf16 v188, v200, v36, v188\n\t"
    "v_dot2_f32_bf16 v189, v200, v200, v189\n\t"
    "s_waitcnt lgkmcnt(2)\n\t"
    "v_dot2_f32_bf16 v190, v204, v36, v190\n\t"
    "v_dot2_f32_bf16 v191, v204, v204, v191\n\t"
    "s_waitcnt lgkmcnt(1)\n\t"
    "v_dot2_f32_bf16 v192, v208, v36, v192\n\t"
    "v_dot2_f32_bf16 v193, v208, v208, v193\n\t"
    "s_waitcnt lgkmcnt(0)\n\t"
    "v_dot2_f32_bf16 v194, v212, v36, v194\n\t"
    "v_dot2_f32_bf16 v195, v212, v212, v195\n\t"
    "v_dot2_f32_bf16 v186, v197, v36, v186\n\t"
    "v_dot2_f32_bf16 v187, v197, v197, v187\n\t"
    "v_dot2_f32_bf16 v188, v201, v36, v188\n\t"
    "v_dot2_f32_bf16 v189, v201, v201, v189\n\t"
    "v_dot2_f32_bf16 v190, v205, v36, v190\n\t"
    "v_dot2_f32_bf16 v191, v205, v205, v191\n\t"
    "v_dot2_f32_bf16 v192, v209, v36, v192\n\t"
    "v_dot2_f32_bf16 v193, v209, v209, v193\n\t"
    "v_dot2_f32_bf16 v194, v213, v36, v194\n\t"
    "v_dot2_f32_bf16 v195, v213, v213, v195\n\t"
    "v_dot2_f32_bf16 v186, v198, v36, v186\n\t"
    "v_dot2_f32_bf16 v187, v198, v198, v187\n\t"
    "v_dot2_f32_bf16 v188, v202, v36, v188\n\t"
    "v_dot2_f32_bf16 v189, v202, v202, v189\n\t"
    "v_dot2_f32_bf16 v190, v206, v36, v190\n\t"
    "v_dot2_f32_bf16 v191, v206, v206, v191\n\t"
    "v_dot2_f32_bf16 v192, v210, v36, v192\n\t"
    "v_dot2_f32_bf16 v193, v210, v210, v193\n\t"
    "v_dot2_f32_bf16 v194, v214, v36, v194\n\t"
    "v_dot2_f32_bf16 v195, v214, v214, v195\n\t"
    "v_dot2_f32_bf16 v186, v199, v36, v186\n\t"
    "v_dot2_f32_bf16 v187, v199, v199, v187\n\t"
    "v_dot2_f32_bf16 v188, v203, v36, v188\n\t"
    "v_dot2_f32_bf16 v189, v203, v203, v189\n\t"
    "v_dot2_f32_bf16 v190, v207, v36, v190\n\t"
    "v_dot2_f32_bf16 v191, v207, v207, v191\n\t"
    "v_dot2_f32_bf16 v192, v211, v36, v192\n\t"
    "v_dot2_f32_bf16 v193, v211, v211, v193\n\t"
    "v_dot2_f32_bf16 v194, v215, v36, v194\n\t"
    "v_dot2_f32_bf16 v195, v215, v215, v195\n\t"
    "s_waitcnt vmcnt(7)\n\t"
    "s_barrier\n\t"
    "v_mfma_f32_16x16x32_bf16 v[134:137], v[110:113], v[74:77], v[134:137]\n\t"
    "ds_read_b64_tr_b16 v[98:99], v34 offset:1024\n\t"
    "s_add_u32 m0, s49, 24576\n\t"
    "s_nop 0\n\t"
    "buffer_load_dwordx4 v13, s[12:15], s57 offen offset:0 lds\n\t"
    "v_add_f32_dpp v186, v186, v186 row_ror:8 row_mask:0xf bank_mask:0xf\n\t"
    "v_add_f32_dpp v187, v187, v187 row_ror:8 row_mask:0xf bank_mask:0xf\n\t"
    "v_mfma_f32_16x16x32_bf16 v[138:141], v[110:113], v[78:81], v[138:141]\n\t"
    "ds_read_b64_tr_b16 v[100:101], v34 offset:1536\n\t"
    "v_add_f32_dpp v188, v188, v188 row_ror:8 row_mask:0xf bank_mask:0xf\n\t"
    "v_mfma_f32_16x16x32_bf16 v[154:157], v[74:77], v[114:117], v[154:157]\n\t"
    "ds_read_b128 v[54:57], v31 offset:0\n\t"
    "v_add_f32_dpp v189, v189, v189 row_ror:8 row_mask:0xf bank_mask:0xf\n\t"
    "v_mfma_f32_16x16x32_bf16 v[158:161], v[78:81], v[114:117], v[158:161]\n\t"
    "ds_read_b128 v[58:61], v31 offset:4096\n\t"
    "buffer_load_dwordx4 v13, s[12:15], s57 offen offset:1024 lds\n\t"
    "v_add_f32_dpp v190, v190, v190 row_ror:8 row_mask:0xf bank_mask:0xf\n\t"
    "v_add_f32_dpp v191, v191, v191 row_ror:8 row_mask:0xf bank_mask:0xf\n\t"
    "v_mfma_f32_16x16x32_bf16 v[142:145], v[110:113], v[82:85], v[142:145]\n\t"
    "ds_read_b64_tr_b16 v[102:103], v34 offset:2048\n\t"
    "v_add_f32_dpp v192, v192, v192 row_ror:8 row_mask:0xf bank_mask:0xf\n\t"
    "v_mfma_f32_16x16x32_bf16 v[162:165], v[82:85], v[114:117], v[162:165]\n\t"
    "ds_read_b64_tr_b16 v[104:105], v34 offset:2560\n\t"
    "v_add_f32_dpp v193, v193, v193 row_ror:8 row_mask:0xf bank_mask:0xf\n\t"
    "v_mfma_f32_16x16x32_bf16 v[174:177], v[106:109], v[36:39], v[174:177]\n\t"
    "ds_read_b128 v[62:65], v31 offset:8192\n\t"
    "buffer_load_dwordx4 v13, s[12:15], s57 offen offset:2048 lds\n\t"
    "v_add_f32_dpp v194, v194, v194 row_ror:8 row_mask:0xf bank_mask:0xf\n\t"
    "v_add_f32_dpp v195, v195, v195 row_ror:8 row_mask:0xf bank_mask:0xf\n\t"
    "v_mfma_f32_16x16x32_bf16 v[118:121], v[106:109], v[78:81], v[118:121]\n\t"
    "ds_read_b64_tr_b16 v[94:95], v34 offset:0\n\t"
    "v_add_f32_dpp v186, v186, v186 row_ror:4 row_mask:0xf bank_mask:0xf\n\t"
    "v_mfma_f32_16x16x32_bf16 v[122:125], v[106:109], v[82:85], v[122:125]\n\t"
    "ds_read_b64_tr_b16 v[96:97], v34 offset:512\n\t"
    "v_add_f32_dpp v187, v187, v187 row_ror:4 row_mask:0xf bank_mask:0xf\n\t"
    "v_mfma_f32_16x16x32_bf16 v[146:149], v[110:113], v[86:89], v[146:149]\n\t"
    "ds_read_b128 v[66:69], v31 offset:12288\n\t"
    "s_add_u32 m0, s49, 27648\n\t"
    "s_nop 0\n\t"
    "buffer_load_dwordx4 v14, s[12:15], s57 offen offset:0 lds\n\t"
    "v_add_f32_dpp v188, v188, v188 row_ror:4 row_mask:0xf bank_mask:0xf\n\t"
    "v_add_f32_dpp v189, v189, v189 row_ror:4 row_mask:0xf bank_mask:0xf\n\t"
    "v_mfma_f32_16x16x32_bf16 v[166:169], v[86:89], v[114:117], v[166:169]\n\t"
    "ds_read_b128 v[70:73], v31 offset:16384\n\t"
    "v_add_f32_dpp v190, v190, v190 row_ror:4 row_mask:0xf bank_mask:0xf\n\t"
    "v_mfma_f32_16x16x32_bf16 v[126:129], v[106:109], v[86:89], v[126:129]\n\t"
    "v_add_f32_dpp v191, v191, v191 row_ror:4 row_mask:0xf bank_mask:0xf\n\t"
    "v_mfma_f32_16x16x32_bf16 v[150:153], v[110:113], v[90:93], v[150:153]\n\t"
    "buffer_load_dwordx4 v14, s[12:15], s57 offen offset:1024 lds\n\t"
    "v_add_f32_dpp v192, v192, v192 row_ror:4 row_mask:0xf bank_mask:0xf\n\t"
    "v_add_f32_dpp v193, v193, v193 row_ror:4 row_mask:0xf bank_mask:0xf\n\t"
    "v_mfma_f32_16x16x32_bf16 v[170:173], v[90:93], v[114:117], v[170:173]\n\t"
    "v_add_f32_dpp v194, v194, v194 row_ror:4 row_mask:0xf bank_mask:0xf\n\t"
    "v_mfma_f32_16x16x32_bf16 v[130:133], v[106:109], v[90:93], v[130:133]\n\t"
    "v_add_f32_dpp v195, v195, v195 row_ror:4 row_mask:0xf bank_mask:0xf\n\t"
    "s_waitcnt lgkmcnt(0)\n\t"
    "v_mfma_f32_16x16x32_bf16 v[134:137], v[98:101], v[54:57], v[134:137]\n\t"
    "ds_read_b64_tr_b16 v[110:111], v34 offset:13312\n\t"
    "buffer_load_dwordx4 v14, s[12:15], s57 offen offset:2048 lds\n\t"
    "v_add_f32_dpp v186, v186, v186 row_ror:2 row_mask:0xf bank_mask:0xf\n\t"
    "v_add_f32_dpp v187, v187, v187 row_ror:2 row_mask:0xf bank_mask:0xf\n\t"
    "v_mfma_f32_16x16x32_bf16 v[138:141], v[98:101], v[58:61], v[138:141]\n\t"
    "ds_read_b64_tr_b16 v[112:113], v34 offset:13824\n\t"
    "v_add_f32_dpp v188, v188, v188 row_ror:2 row_mask:0xf bank_mask:0xf\n\t"
    "v_mfma_f32_16x16x32_bf16 v[154:157], v[54:57], v[102:105], v[154:157]\n\t"
    "ds_read_b128 v[74:77], v32 offset:0\n\t"
    "v_add_f32_dpp v189, v189, v189 row_ror:2 row_mask:0xf bank_mask:0xf\n\t"
    "v_mfma_f32_16x16x32_bf16 v[158:161], v[58:61], v[102:105], v[158:161]\n\t"
    "ds_read_b128 v[78:81], v32 offset:4096\n\t"
    "v_add_f32_dpp v190, v190, v190 row_ror:2 row_mask:0xf bank_mask:0xf\n\t"
    "v_add_f32_dpp v191, v191, v191 row_ror:2 row_mask:0xf bank_mask:0xf\n\t"
    "v_mfma_f32_16x16x32_bf16 v[142:145], v[98:101], v[62:65], v[142:145]\n\t"
    "ds_read_b64_tr_b16 v[114:115], v34 offset:14336\n\t"
    "v_add_f32_dpp v192, v192, v192 row_ror:2 row_mask:0xf bank_mask:0xf\n\t"
    "v_mfma_f32_16x16x32_bf16 v[162:165], v[62:65], v[102:105], v[162:165]\n\t"
    "ds_read_b64_tr_b16 v[116:117], v34 offset:14848\n\t"
    "v_add_f32_dpp v193, v193, v193 row_ror:2 row_mask:0xf bank_mask:0xf\n\t"
    "v_mfma_f32_16x16x32_bf16 v[174:177], v[94:97], v[36:39], v[174:177]\n\t"
    "ds_read_b128 v[82:85], v32 offset:8192\n\t"
    "v_add_f32_dpp v194, v194, v194 row_ror:2 row_mask:0xf bank_mask:0xf\n\t"
    "v_add_f32_dpp v195, v195, v195 row_ror:2 row_mask:0xf bank_mask:0xf\n\t"
    "v_mfma_f32_16x16x32_bf16 v[118:121], v[94:97], v[58:61], v[118:121]\n\t"
    "ds_read_b64_tr_b16 v[106:107], v34 offset:12288\n\t"
    "v_add_f32_dpp v186, v186, v186 row_ror:1 row_mask:0xf bank_mask:0xf\n\t"
    "v_mfma_f32_16x16x32_bf16 v[122:125], v[94:97], v[62:65], v[122:125]\n\t"
    "ds_read_b64_tr_b16 v[108:109], v34 offset:12800\n\t"
    "v_add_f32_dpp v187, v187, v187 row_ror:1 row_mask:0xf bank_mask:0xf\n\t"
    "v_mfma_f32_16x16x32_bf16 v[146:149], v[98:101], v[66:69], v[146:149]\n\t"
    "ds_read_b128 v[86:89], v32 offset:12288\n\t"
    "v_add_f32_dpp v188, v188, v188 row_ror:1 row_mask:0xf bank_mask:0xf\n\t"
    "v_add_f32_dpp v189, v189, v189 row_ror:1 row_mask:0xf bank_mask:0xf\n\t"
    "v_mfma_f32_16x16x32_bf16 v[166:169], v[66:69], v[102:105], v[166:169]\n\t"
    "ds_read_b128 v[90:93], v32 offset:16384\n\t"
    "v_add_f32_dpp v190, v190, v190 row_ror:1 row_mask:0xf bank_mask:0xf\n\t"
    "v_mfma_f32_16x16x32_bf16 v[126:129], v[94:97], v[66:69], v[126:129]\n\t"
    "v_add_f32_dpp v191, v191, v191 row_ror:1 row_mask:0xf bank_mask:0xf\n\t"
    "v_mfma_f32_16x16x32_bf16 v[150:153], v[98:101], v[70:73], v[150:153]\n\t"
    "v_add_f32_dpp v192, v192, v192 row_ror:1 row_mask:0xf bank_mask:0xf\n\t"
    "v_add_f32_dpp v193, v193, v193 row_ror:1 row_mask:0xf bank_mask:0xf\n\t"
    "v_mfma_f32_16x16x32_bf16 v[170:173], v[70:73], v[102:105], v[170:173]\n\t"
    "v_add_f32_dpp v194, v194, v194 row_ror:1 row_mask:0xf bank_mask:0xf\n\t"
    "v_mfma_f32_16x16x32_bf16 v[130:133], v[94:97], v[70:73], v[130:133]\n\t"
    "v_add_f32_dpp v195, v195, v195 row_ror:1 row_mask:0xf bank_mask:0xf\n\t"
    "s_waitcnt lgkmcnt(0)\n\t"
    "s_waitcnt vmcnt(7)\n\t"
    "s_barrier\n\t"
    "v_mfma_f32_16x16x32_bf16 v[134:137], v[110:113], v[74:77], v[134:137]\n\t"
    "ds_read_b64_tr_b16 v[98:99], v33 offset:1024\n\t"
    "s_add_u32 m0, s63, 65536\n\t"
    "s_lshl_b32 s78, s87, 14\n\t"
    "buffer_load_dwordx4 v51, s[16:19], s78 offen offset:0 lds\n\t"
    "v_cmp_eq_u32 vcc, 1, v11\n\t"
    "s_nop 1\n\t"
    "v_cndmask_b32 v186, v186, v188, vcc\n\t"
    "v_mfma_f32_16x16x32_bf16 v[138:141], v[110:113], v[78:81], v[138:141]\n\t"
    "ds_read_b64_tr_b16 v[100:101], v33 offset:1536\n\t"
    "v_cndmask_b32 v187, v187, v189, vcc\n\t"
    "v_mfma_f32_16x16x32_bf16 v[154:157], v[74:77], v[114:117], v[154:157]\n\t"
    "ds_read_b128 v[54:57], v29 offset:20480\n\t"
    "v_cmp_eq_u32 vcc, 2, v11\n\t"
    "v_mfma_f32_16x16x32_bf16 v[158:161], v[78:81], v[114:117], v[158:161]\n\t"
    "ds_read_b128 v[58:61], v29 offset:24576\n\t"
    "buffer_load_dwordx4 v52, s[16:19], s78 offen offset:1024 lds\n\t"
    "s_add_u32 s78, s78, 16384\n\t"
    "s_bitset0_b32 s78, 17\n\t"
    "v_cndmask_b32 v186, v186, v190, vcc\n\t"
    "v_cndmask_b32 v187, v187, v191, vcc\n\t"
    "v_mfma_f32_16x16x32_bf16 v[142:145], v[110:113], v[82:85], v[142:145]\n\t"
    "ds_read_b64_tr_b16 v[102:103], v33 offset:2048\n\t"
    "v_cmp_eq_u32 vcc, 3, v11\n\t"
    "v_mfma_f32_16x16x32_bf16 v[162:165], v[82:85], v[114:117], v[162:165]\n\t"
    "ds_read_b64_tr_b16 v[104:105], v33 offset:2560\n\t"
    "v_cndmask_b32 v186, v186, v192, vcc\n\t"
    "v_mfma_f32_16x16x32_bf16 v[174:177], v[106:109], v[36:39], v[174:177]\n\t"
    "ds_read_b128 v[62:65], v29 offset:28672\n\t"
    "v_cndmask_b32 v187, v187, v193, vcc\n\t"
    "v_cmp_eq_u32 vcc, 4, v11\n\t"
    "v_mfma_f32_16x16x32_bf16 v[118:121], v[106:109], v[78:81], v[118:121]\n\t"
    "ds_read_b64_tr_b16 v[94:95], v33 offset:0\n\t"
    "s_add_u32 m0, s63, 67584\n\t"
    "s_nop 0\n\t"
    "buffer_load_dwordx4 v51, s[16:19], s78 offen offset:0 lds\n\t"
    "v_cndmask_b32 v186, v186, v194, vcc\n\t"
    "v_mfma_f32_16x16x32_bf16 v[122:125], v[106:109], v[82:85], v[122:125]\n\t"
    "ds_read_b64_tr_b16 v[96:97], v33 offset:512\n\t"
    "v_cndmask_b32 v187, v187, v195, vcc\n\t"
    "v_mfma_f32_16x16x32_bf16 v[146:149], v[110:113], v[86:89], v[146:149]\n\t"
    "ds_read_b128 v[66:69], v29 offset:32768\n\t"
    "v_mul_f32 v186, 0x3b000000, v186\n\t"
    "v_mul_f32 v188, v186, v186\n\t"
    "v_mfma_f32_16x16x32_bf16 v[166:169], v[86:89], v[114:117], v[166:169]\n\t"
    "ds_read_b128 v[70:73], v29 offset:36864\n\t"
    "v_mov_b32 v189, 0x3b000000\n\t"
    "v_mfma_f32_16x16x32_bf16 v[126:129], v[106:109], v[86:89], v[126:129]\n\t"
    "buffer_load_dwordx4 v52, s[16:19], s78 offen offset:1024 lds\n\t"
    "s_add_u32 s78, s78, 16384\n\t"
    "s_bitset0_b32 s78, 17\n\t"
    "v_fma_f32 v187, v187, v189, -v188\n\t"
    "v_mfma_f32_16x16x32_bf16 v[150:153], v[110:113], v[90:93], v[150:153]\n\t"
    "v_max_f32 v187, 0, v187\n\t"
    "v_add_f32 v187, 0x3727c5ac, v187\n\t"
    "v_mfma_f32_16x16x32_bf16 v[170:173], v[90:93], v[114:117], v[170:173]\n\t"
    "v_rsq_f32 v187, v187\n\t"
    "v_mfma_f32_16x16x32_bf16 v[130:133], v[106:109], v[90:93], v[130:133]\n\t"
    "s_mul_i32 s40, s33, 80\n\t"
    "s_waitcnt lgkmcnt(0)\n\t"
    "v_mfma_f32_16x16x32_bf16 v[134:137], v[98:101], v[54:57], v[134:137]\n\t"
    "ds_read_b64_tr_b16 v[110:111], v33 offset:13312\n\t"
    "s_add_u32 m0, s63, 69632\n\t"
    "s_nop 0\n\t"
    "buffer_load_dwordx4 v51, s[16:19], s78 offen offset:0 lds\n\t"
    "v_lshl_add_u32 v188, v11, 2, v12\n\t"
    "v_lshlrev_b32 v188, 2, v188\n\t"
    "v_mfma_f32_16x16x32_bf16 v[138:141], v[98:101], v[58:61], v[138:141]\n\t"
    "ds_read_b64_tr_b16 v[112:113], v33 offset:13824\n\t"
    "v_add_u32 v189, s40, v188\n\t"
    "v_mfma_f32_16x16x32_bf16 v[154:157], v[54:57], v[102:105], v[154:157]\n\t"
    "ds_read_b128 v[74:77], v30 offset:20480\n\t"
    "v_add_u32 v188, 1024, v188\n\t"
    "v_mfma_f32_16x16x32_bf16 v[158:161], v[58:61], v[102:105], v[158:161]\n\t"
    "ds_read_b128 v[78:81], v30 offset:24576\n\t"
    "buffer_load_dwordx4 v52, s[16:19], s78 offen offset:1024 lds\n\t"
    "s_add_u32 s78, s78, 16384\n\t"
    "s_bitset0_b32 s78, 17\n\t"
    "v_cmp_gt_u32 vcc, 5, v11\n\t"
    "s_nop 1\n\t"
    "v_cndmask_b32 v188, v188, v189, vcc\n\t"
    "v_mfma_f32_16x16x32_bf16 v[142:145], v[98:101], v[62:65], v[142:145]\n\t"
    "ds_read_b64_tr_b16 v[114:115], v33 offset:14336\n\t"
    "v_add_u32 v188, 155648, v188\n\t"
    "v_mfma_f32_16x16x32_bf16 v[162:165], v[62:65], v[102:105], v[162:165]\n\t"
    "ds_read_b64_tr_b16 v[116:117], v33 offset:14848\n\t"
    "ds_write2_b32 v188, v186, v187 offset0:0 offset1:80\n\t"
    "v_mfma_f32_16x16x32_bf16 v[174:177], v[94:97], v[36:39], v[174:177]\n\t"
    "ds_read_b128 v[82:85], v30 offset:28672\n\t"
    "v_mfma_f32_16x16x32_bf16 v[118:121], v[94:97], v[58:61], v[118:121]\n\t"
    "ds_read_b64_tr_b16 v[106:107], v33 offset:12288\n\t"
    "s_add_u32 m0, s63, 71680\n\t"
    "s_nop 0\n\t"
    "buffer_load_dwordx4 v51, s[16:19], s78 offen offset:0 lds\n\t"
    "v_mfma_f32_16x16x32_bf16 v[122:125], v[94:97], v[62:65], v[122:125]\n\t"
    "ds_read_b64_tr_b16 v[108:109], v33 offset:12800\n\t"
    "v_mfma_f32_16x16x32_bf16 v[146:149], v[98:101], v[66:69], v[146:149]\n\t"
    "ds_read_b128 v[86:89], v30 offset:32768\n\t"
    "v_mfma_f32_16x16x32_bf16 v[166:169], v[66:69], v[102:105], v[166:169]\n\t"
    "ds_read_b128 v[90:93], v30 offset:36864\n\t"
    "v_mfma_f32_16x16x32_bf16 v[126:129], v[94:97], v[66:69], v[126:129]\n\t"
    "buffer_load_dwordx4 v52, s[16:19], s78 offen offset:1024 lds\n\t"
    "s_add_u32 s78, s78, 16384\n\t"
    "s_bitset0_b32 s78, 17\n\t"
    "v_mfma_f32_16x16x32_bf16 v[150:153], v[98:101], v[70:73], v[150:153]\n\t"
    "v_mfma_f32_16x16x32_bf16 v[170:173], v[70:73], v[102:105], v[170:173]\n\t"
    "v_mfma_f32_16x16x32_bf16 v[130:133], v[94:97], v[70:73], v[130:133]\n\t"
    "s_waitcnt lgkmcnt(0)\n\t"
    "s_waitcnt vmcnt(8)\n\t"
    "s_barrier\n\t"
    "v_mfma_f32_16x16x32_bf16 v[134:137], v[110:113], v[74:77], v[134:137]\n\t"
    "ds_read_b64_tr_b16 v[98:99], v33 offset:25600\n\t"
    "s_add_u32 m0, s63, 98304\n\t"
    "s_nop 0\n\t"
    "buffer_load_dwordx4 v51, s[16:19], s78 offen offset:0 lds\n\t"
    "v_lshlrev_b32 v7, 2, v11\n\t"
    "v_mfma_f32_16x16x32_bf16 v[138:141], v[110:113], v[78:81], v[138:141]\n\t"
    "ds_read_b64_tr_b16 v[100:101], v33 offset:26112\n\t"
    "v_add_u32 v7, 155648, v7\n\t"
    "v_mfma_f32_16x16x32_bf16 v[154:157], v[74:77], v[114:117], v[154:157]\n\t"
    "ds_read_b128 v[54:57], v31 offset:20480\n\t"
    "v_lshlrev_b32 v8, 4, v12\n\t"
    "v_mfma_f32_16x16x32_bf16 v[158:161], v[78:81], v[114:117], v[158:161]\n\t"
    "ds_read_b128 v[58:61], v31 offset:24576\n\t"
    "buffer_load_dwordx4 v52, s[16:19], s78 offen offset:1024 lds\n\t"
    "s_add_u32 s78, s78, 16384\n\t"
    "s_bitset0_b32 s78, 17\n\t"
    "v_add_u32 v8, 155648, v8\n\t"
    "v_mfma_f32_16x16x32_bf16 v[142:145], v[110:113], v[82:85], v[142:145]\n\t"
    "ds_read_b64_tr_b16 v[102:103], v33 offset:26624\n\t"
    "v_mfma_f32_16x16x32_bf16 v[162:165], v[82:85], v[114:117], v[162:165]\n\t"
    "ds_read_b64_tr_b16 v[104:105], v33 offset:27136\n\t"
    "v_mfma_f32_16x16x32_bf16 v[174:177], v[106:109], v[36:39], v[174:177]\n\t"
    "ds_read_b128 v[62:65], v31 offset:28672\n\t"
    "v_mfma_f32_16x16x32_bf16 v[118:121], v[106:109], v[78:81], v[118:121]\n\t"
    "ds_read_b64_tr_b16 v[94:95], v33 offset:24576\n\t"
    "s_add_u32 m0, s63, 100352\n\t"
    "s_nop 0\n\t"
    "buffer_load_dwordx4 v51, s[16:19], s78 offen offset:0 lds\n\t"
    "v_mfma_f32_16x16x32_bf16 v[122:125], v[106:109], v[82:85], v[122:125]\n\t"
    "ds_read_b64_tr_b16 v[96:97], v33 offset:25088\n\t"
    "v_mfma_f32_16x16x32_bf16 v[146:149], v[110:113], v[86:89], v[146:149]\n\t"
    "ds_read_b128 v[66:69], v31 offset:32768\n\t"
    "v_mfma_f32_16x16x32_bf16 v[166:169], v[86:89], v[114:117], v[166:169]\n\t"
    "ds_read_b128 v[70:73], v31 offset:36864\n\t"
    "v_mfma_f32_16x16x32_bf16 v[126:129], v[106:109], v[86:89], v[126:129]\n\t"
    "ds_read2_b32 v[222:223], v7 offset0:0 offset1:80\n\t"
    "ds_read_b128 v[2:5], v8 offset:0\n\t"
    "buffer_load_dwordx4 v52, s[16:19], s78 offen offset:1024 lds\n\t"
    "s_add_u32 s78, s78, 16384\n\t"
    "s_bitset0_b32 s78, 17\n\t"
    "v_mfma_f32_16x16x32_bf16 v[150:153], v[110:113], v[90:93], v[150:153]\n\t"
    "ds_read_b128 v[202:205], v8 offset:320\n\t"
    "v_mfma_f32_16x16x32_bf16 v[170:173], v[90:93], v[114:117], v[170:173]\n\t"
    "ds_read2_b32 v[224:225], v7 offset0:16 offset1:96\n\t"
    "ds_read_b128 v[186:189], v8 offset:64\n\t"
    "v_mfma_f32_16x16x32_bf16 v[130:133], v[106:109], v[90:93], v[130:133]\n\t"
    "ds_read_b128 v[206:209], v8 offset:384\n\t"
    "s_waitcnt lgkmcnt(6)\n\t"
    "v_mfma_f32_16x16x32_bf16 v[134:137], v[98:101], v[54:57], v[134:137]\n\t"
    "ds_read_b64_tr_b16 v[110:111], v33 offset:37888\n\t"
    "s_add_u32 m0, s63, 102400\n\t"
    "s_nop 0\n\t"
    "buffer_load_dwordx4 v51, s[16:19], s78 offen offset:0 lds\n\t"
    "v_mfma_f32_16x16x32_bf16 v[138:141], v[98:101], v[58:61], v[138:141]\n\t"
    "ds_read_b64_tr_b16 v[112:113], v33 offset:38400\n\t"
    "v_mfma_f32_16x16x32_bf16 v[154:157], v[54:57], v[102:105], v[154:157]\n\t"
    "ds_read_b128 v[74:77], v32 offset:20480\n\t"
    "v_mfma_f32_16x16x32_bf16 v[158:161], v[58:61], v[102:105], v[158:161]\n\t"
    "ds_read_b128 v[78:81], v32 offset:24576\n\t"
    "buffer_load_dwordx4 v52, s[16:19], s78 offen offset:1024 lds\n\t"
    "s_add_u32 s78, s78, 16384\n\t"
    "s_bitset0_b32 s78, 17\n\t"
    "v_mfma_f32_16x16x32_bf16 v[142:145], v[98:101], v[62:65], v[142:145]\n\t"
    "ds_read_b64_tr_b16 v[114:115], v33 offset:38912\n\t"
    "v_mfma_f32_16x16x32_bf16 v[162:165], v[62:65], v[102:105], v[162:165]\n\t"
    "ds_read_b64_tr_b16 v[116:117], v33 offset:39424\n\t"
    "v_mfma_f32_16x16x32_bf16 v[174:177], v[94:97], v[36:39], v[174:177]\n\t"
    "ds_read_b128 v[82:85], v32 offset:28672\n\t"
    "v_mfma_f32_16x16x32_bf16 v[118:121], v[94:97], v[58:61], v[118:121]\n\t"
    "ds_read_b64_tr_b16 v[106:107], v33 offset:36864\n\t"
    "s_add_u32 m0, s63, 104448\n\t"
    "s_nop 0\n\t"
    "buffer_load_dwordx4 v51, s[16:19], s78 offen offset:0 lds\n\t"
    "v_mfma_f32_16x16x32_bf16 v[122:125], v[94:97], v[62:65], v[122:125]\n\t"
    "ds_read_b64_tr_b16 v[108:109], v33 offset:37376\n\t"
    "v_mfma_f32_16x16x32_bf16 v[146:149], v[98:101], v[66:69], v[146:149]\n\t"
    "ds_read_b128 v[86:89], v32 offset:32768\n\t"
    "v_mfma_f32_16x16x32_bf16 v[166:169], v[66:69], v[102:105], v[166:169]\n\t"
    "ds_read_b128 v[90:93], v32 offset:36864\n\t"
    "v_mfma_f32_16x16x32_bf16 v[126:129], v[94:97], v[66:69], v[126:129]\n\t"
    "ds_read2_b32 v[226:227], v7 offset0:32 offset1:112\n\t"
    "buffer_load_dwordx4 v52, s[16:19], s78 offen offset:1024 lds\n\t"
    "v_mfma_f32_16x16x32_bf16 v[150:153], v[98:101], v[70:73], v[150:153]\n\t"
    "ds_read_b128 v[190:193], v8 offset:128\n\t"
    "v_mfma_f32_16x16x32_bf16 v[170:173], v[70:73], v[102:105], v[170:173]\n\t"
    "ds_read_b128 v[210:213], v8 offset:448\n\t"
    "v_mfma_f32_16x16x32_bf16 v[130:133], v[94:97], v[70:73], v[130:133]\n\t"
    "s_waitcnt lgkmcnt(3)\n\t"
    "s_barrier\n\t"
    "v_mfma_f32_16x16x32_bf16 v[134:137], v[110:113], v[74:77], v[134:137]\n\t"
    "ds_read2_b32 v[228:229], v7 offset0:48 offset1:128\n\t"
    "s_mov_b32 m0, s63\n\t"
    "s_mov_b32 s42, 15360\n\t"
    "buffer_load_dwordx4 v28, s[8:11], 0 offen lds\n\t"
    "v_mfma_f32_16x16x32_bf16 v[138:141], v[110:113], v[78:81], v[138:141]\n\t"
    "ds_read_b128 v[194:197], v8 offset:192\n\t"
    "v_mfma_f32_16x16x32_bf16 v[154:157], v[74:77], v[114:117], v[154:157]\n\t"
    "ds_read_b128 v[214:217], v8 offset:512\n\t"
    "v_mfma_f32_16x16x32_bf16 v[158:161], v[78:81], v[114:117], v[158:161]\n\t"
    "ds_read2_b32 v[230:231], v7 offset0:64 offset1:144\n\t"
    "buffer_load_dwordx4 v28, s[8:11], s42 offen offset:1024 lds\n\t"
    "v_mfma_f32_16x16x32_bf16 v[142:145], v[110:113], v[82:85], v[142:145]\n\t"
    "ds_read_b128 v[198:201], v8 offset:256\n\t"
    "v_mfma_f32_16x16x32_bf16 v[162:165], v[82:85], v[114:117], v[162:165]\n\t"
    "ds_read_b128 v[218:221], v8 offset:576\n\t"
    "v_mfma_f32_16x16x32_bf16 v[174:177], v[106:109], v[36:39], v[174:177]\n\t"
    "v_mfma_f32_16x16x32_bf16 v[118:121], v[106:109], v[78:81], v[118:121]\n\t"
    "s_add_u32 m0, s63, 2048\n\t"
    "s_add_u32 s42, s79, 0\n\t"
    "buffer_load_dwordx4 v42, s[20:23], s42 offen lds\n\t"
    "v_mfma_f32_16x16x32_bf16 v[122:125], v[106:109], v[82:85], v[122:125]\n\t"
    "v_mfma_f32_16x16x32_bf16 v[146:149], v[110:113], v[86:89], v[146:149]\n\t"
    "v_mfma_f32_16x16x32_bf16 v[166:169], v[86:89], v[114:117], v[166:169]\n\t"
    "v_mfma_f32_16x16x32_bf16 v[126:129], v[106:109], v[86:89], v[126:129]\n\t"
    "s_add_u32 m0, s63, 3072\n\t"
    "s_nop 0\n\t"
    "buffer_load_dwordx4 v42, s[24:27], s42 offen lds\n\t"
    "v_mfma_f32_16x16x32_bf16 v[150:153], v[110:113], v[90:93], v[150:153]\n\t"
    "v_mfma_f32_16x16x32_bf16 v[170:173], v[90:93], v[114:117], v[170:173]\n\t"
    "v_mfma_f32_16x16x32_bf16 v[130:133], v[106:109], v[90:93], v[130:133]\n\t"
    "s_load_dwordx2 s[8:9], %[ka], 40\n\t"
    "s_load_dwordx2 s[12:13], %[ka], 48\n\t"
    "s_mov_b32 s10, 1835008\n\t"
    "s_mov_b32 s14, 2097152\n\t"
    "v_mov_b32_dpp v178, v134 row_newbcast:0 row_mask:0xf bank_mask:0xf\n\t"
    "s_add_u32 m0, s63, 4096\n\t"
    "s_add_u32 s42, s79, 57344\n\t"
    "buffer_load_dwordx4 v42, s[20:23], s42 offen lds\n\t"
    "v_mov_b32_dpp v179, v135 row_newbcast:0 row_mask:0xf bank_mask:0xf\n\t"
    "v_mov_b32_dpp v180, v136 row_newbcast:0 row_mask:0xf bank_mask:0xf\n\t"
    "v_mov_b32_dpp v181, v137 row_newbcast:0 row_mask:0xf bank_mask:0xf\n\t"
    "v_mov_b32 v182, v154\n\t"
    "v_mov_b32 v183, v154\n\t"
    "s_nop 1\n\t"
    "v_permlane16_swap_b32 v182, v183\n\t"
    "v_mov_b32 v183, v182\n\t"
    "s_nop 1\n\t"
    "v_permlane32_swap_b32 v182, v183\n\t"
    "v_mov_b32 v183, v182\n\t"
    "s_add_u32 m0, s63, 5120\n\t"
    "s_nop 0\n\t"
    "buffer_load_dwordx4 v42, s[24:27], s42 offen lds\n\t"
    "v_pk_fma_f32 v[134:135], v[222:223], v[178:179], v[134:135] op_sel_hi:[0,1,1] neg_lo:[1,0,0] neg_hi:[1,0,0]\n\t"
    "v_pk_fma_f32 v[136:137], v[222:223], v[180:181], v[136:137] op_sel_hi:[0,1,1] neg_lo:[1,0,0] neg_hi:[1,0,0]\n\t"
    "v_pk_mul_f32 v[134:135], v[134:135], v[222:223] op_sel:[0,1] op_sel_hi:[1,1]\n\t"
    "v_pk_mul_f32 v[136:137], v[136:137], v[222:223] op_sel:[0,1] op_sel_hi:[1,1]\n\t"
    "v_cvt_pk_bf16_f32 v54, v134, v135\n\t"
    "v_cvt_pk_bf16_f32 v55, v136, v137\n\t"
    "v_pk_fma_f32 v[138:139], v[224:225], v[178:179], v[138:139] op_sel_hi:[0,1,1] neg_lo:[1,0,0] neg_hi:[1,0,0]\n\t"
    "v_pk_fma_f32 v[140:141], v[224:225], v[180:181], v[140:141] op_sel_hi:[0,1,1] neg_lo:[1,0,0] neg_hi:[1,0,0]\n\t"
    "v_pk_fma_f32 v[118:119], v[224:225], v[174:175], v[118:119] op_sel_hi:[0,1,1] neg_lo:[1,0,0] neg_hi:[1,0,0]\n\t"
    "v_pk_fma_f32 v[120:121], v[224:225], v[176:177], v[120:121] op_sel_hi:[0,1,1] neg_lo:[1,0,0] neg_hi:[1,0,0]\n\t"
    "s_add_u32 m0, s63, 6144\n\t"
    "s_add_u32 s42, s79, 114688\n\t"
    "buffer_load_dwordx4 v42, s[20:23], s42 offen lds\n\t"
    "v_pk_mul_f32 v[138:139], v[138:139], v[224:225] op_sel:[0,1] op_sel_hi:[1,1]\n\t"
    "v_pk_mul_f32 v[140:141], v[140:141], v[224:225] op_sel:[0,1] op_sel_hi:[1,1]\n\t"
    "v_mul_f32 v225, 0x3eb8aa3b, v225\n\t"
    "v_pk_mul_f32 v[118:119], v[118:119], v[224:225] op_sel:[0,1] op_sel_hi:[1,1]\n\t"
    "v_pk_mul_f32 v[120:121], v[120:121], v[224:225] op_sel:[0,1] op_sel_hi:[1,1]\n\t"
    "v_cvt_pk_bf16_f32 v56, v138, v139\n\t"
    "v_cvt_pk_bf16_f32 v57, v140, v141\n\t"
    "v_cvt_pk_bf16_f32 v74, v118, v119\n\t"
    "v_cvt_pk_bf16_f32 v75, v120, v121\n\t"
    "s_waitcnt lgkmcnt(0)\n\t"
    "v_pk_fma_f32 v[142:143], v[226:227], v[178:179], v[142:143] op_sel_hi:[0,1,1] neg_lo:[1,0,0] neg_hi:[1,0,0]\n\t"
    "s_add_u32 m0, s63, 7168\n\t"
    "s_nop 0\n\t"
    "buffer_load_dwordx4 v42, s[24:27], s42 offen lds\n\t"
    "v_pk_fma_f32 v[144:145], v[226:227], v[180:181], v[144:145] op_sel_hi:[0,1,1] neg_lo:[1,0,0] neg_hi:[1,0,0]\n\t"
    "v_pk_fma_f32 v[122:123], v[226:227], v[174:175], v[122:123] op_sel_hi:[0,1,1] neg_lo:[1,0,0] neg_hi:[1,0,0]\n\t"
    "v_pk_fma_f32 v[124:125], v[226:227], v[176:177], v[124:125] op_sel_hi:[0,1,1] neg_lo:[1,0,0] neg_hi:[1,0,0]\n\t"
    "v_pk_mul_f32 v[142:143], v[142:143], v[226:227] op_sel:[0,1] op_sel_hi:[1,1]\n\t"
    "v_pk_mul_f32 v[144:145], v[144:145], v[226:227] op_sel:[0,1] op_sel_hi:[1,1]\n\t"
    "v_mul_f32 v227, 0x3eb8aa3b, v227\n\t"
    "v_pk_mul_f32 v[122:123], v[122:123], v[226:227] op_sel:[0,1] op_sel_hi:[1,1]\n\t"
    "v_pk_mul_f32 v[124:125], v[124:125], v[226:227] op_sel:[0,1] op_sel_hi:[1,1]\n\t"
    "v_cvt_pk_bf16_f32 v58, v142, v143\n\t"
    "s_add_u32 m0, s63, 32768\n\t"
    "s_add_u32 s42, s79, 172032\n\t"
    "buffer_load_dwordx4 v42, s[20:23], s42 offen lds\n\t"
    "v_cvt_pk_bf16_f32 v59, v144, v145\n\t"
    "v_cvt_pk_bf16_f32 v76, v122, v123\n\t"
    "v_cvt_pk_bf16_f32 v77, v124, v125\n\t"
    "v_pk_fma_f32 v[146:147], v[228:229], v[178:179], v[146:147] op_sel_hi:[0,1,1] neg_lo:[1,0,0] neg_hi:[1,0,0]\n\t"
    "v_pk_fma_f32 v[148:149], v[228:229], v[180:181], v[148:149] op_sel_hi:[0,1,1] neg_lo:[1,0,0] neg_hi:[1,0,0]\n\t"
    "v_pk_fma_f32 v[126:127], v[228:229], v[174:175], v[126:127] op_sel_hi:[0,1,1] neg_lo:[1,0,0] neg_hi:[1,0,0]\n\t"
    "v_pk_fma_f32 v[128:129], v[228:229], v[176:177], v[128:129] op_sel_hi:[0,1,1] neg_lo:[1,0,0] neg_hi:[1,0,0]\n\t"
    "v_pk_mul_f32 v[146:147], v[146:147], v[228:229] op_sel:[0,1] op_sel_hi:[1,1]\n\t"
    "v_pk_mul_f32 v[148:149], v[148:149], v[228:229] op_sel:[0,1] op_sel_hi:[1,1]\n\t"
    "v_mul_f32 v229, 0x3eb8aa3b, v229\n\t"
    "s_add_u32 m0, s63, 33792\n\t"
    "s_nop 0\n\t"
    "buffer_load_dwordx4 v42, s[24:27], s42 offen lds\n\t"
    "v_pk_mul_f32 v[126:127], v[126:127], v[228:229] op_sel:[0,1] op_sel_hi:[1,1]\n\t"
    "v_pk_mul_f32 v[128:129], v[128:129], v[228:229] op_sel:[0,1] op_sel_hi:[1,1]\n\t"
    "v_cvt_pk_bf16_f32 v60, v146, v147\n\t"
    "v_cvt_pk_bf16_f32 v61, v148, v149\n\t"
    "v_cvt_pk_bf16_f32 v78, v126, v127\n\t"
    "v_cvt_pk_bf16_f32 v79, v128, v129\n\t"
    "v_pk_fma_f32 v[150:151], v[230:231], v[178:179], v[150:151] op_sel_hi:[0,1,1] neg_lo:[1,0,0] neg_hi:[1,0,0]\n\t"
    "v_pk_fma_f32 v[152:153], v[230:231], v[180:181], v[152:153] op_sel_hi:[0,1,1] neg_lo:[1,0,0] neg_hi:[1,0,0]\n\t"
    "v_pk_fma_f32 v[130:131], v[230:231], v[174:175], v[130:131] op_sel_hi:[0,1,1] neg_lo:[1,0,0] neg_hi:[1,0,0]\n\t"
    "v_pk_fma_f32 v[132:133], v[230:231], v[176:177], v[132:133] op_sel_hi:[0,1,1] neg_lo:[1,0,0] neg_hi:[1,0,0]\n\t"
    "s_add_u32 m0, s63, 34816\n\t"
    "s_add_u32 s42, s80, 0\n\t"
    "buffer_load_dwordx4 v42, s[20:23], s42 offen lds\n\t"
    "v_pk_mul_f32 v[150:151], v[150:151], v[230:231] op_sel:[0,1] op_sel_hi:[1,1]\n\t"
    "v_pk_mul_f32 v[152:153], v[152:153], v[230:231] op_sel:[0,1] op_sel_hi:[1,1]\n\t"
    "v_mul_f32 v231, 0x3eb8aa3b, v231\n\t"
    "v_pk_mul_f32 v[130:131], v[130:131], v[230:231] op_sel:[0,1] op_sel_hi:[1,1]\n\t"
    "v_pk_mul_f32 v[132:133], v[132:133], v[230:231] op_sel:[0,1] op_sel_hi:[1,1]\n\t"
    "v_cvt_pk_bf16_f32 v62, v150, v151\n\t"
    "v_cvt_pk_bf16_f32 v63, v152, v153\n\t"
    "v_cvt_pk_bf16_f32 v80, v130, v131\n\t"
    "v_cvt_pk_bf16_f32 v81, v132, v133\n\t"
    "s_add_u32 m0, s63, 35840\n\t"
    "s_nop 0\n\t"
    "buffer_load_dwordx4 v42, s[24:27], s42 offen lds\n\t"
    "v_mfma_f32_16x16x16_bf16 v[138:141], v[54:55], v[74:75], 0\n\t"
    "v_pk_fma_f32 v[154:155], v[2:3], v[182:183], v[154:155] neg_lo:[1,0,0] neg_hi:[1,0,0]\n\t"
    "v_pk_fma_f32 v[156:157], v[4:5], v[182:183], v[156:157] neg_lo:[1,0,0] neg_hi:[1,0,0]\n\t"
    "s_waitcnt vmcnt(12)\n\t"
    "ds_read_b64_tr_b16 v[30:31], v49 offset:0\n\t"
    "ds_read_b64_tr_b16 v[32:33], v49 offset:512\n\t"
    "v_mfma_f32_16x16x16_bf16 v[118:121], v[56:57], v[74:75], 0\n\t"
    "v_pk_mul_f32 v[154:155], v[154:155], v[202:203]\n\t"
    "v_pk_mul_f32 v[156:157], v[156:157], v[204:205]\n\t"
    "v_mfma_f32_16x16x16_bf16 v[142:145], v[56:57], v[76:77], 0\n\t"
    "v_cvt_pk_bf16_f32 v64, v154, v155\n\t"
    "v_cvt_pk_bf16_f32 v65, v156, v157\n\t"
    "ds_read_b64_tr_b16 v[110:111], v50 offset:0\n\t"
    "ds_read_b64_tr_b16 v[112:113], v50 offset:512\n\t"
    "v_mfma_f32_16x16x16_bf16 v[122:125], v[58:59], v[76:77], 0\n\t"
    "s_add_u32 m0, s63, 36864\n\t"
    "s_add_u32 s42, s80, 57344\n\t"
    "buffer_load_dwordx4 v42, s[20:23], s42 offen lds\n\t"
    "v_pk_fma_f32 v[158:159], v[186:187], v[182:183], v[158:159] neg_lo:[1,0,0] neg_hi:[1,0,0]\n\t"
    "v_pk_fma_f32 v[160:161], v[188:189], v[182:183], v[160:161] neg_lo:[1,0,0] neg_hi:[1,0,0]\n\t"
    "v_mfma_f32_16x16x16_bf16 v[146:149], v[58:59], v[78:79], 0\n\t"
    "v_pk_mul_f32 v[158:159], v[158:159], v[206:207]\n\t"
    "v_pk_mul_f32 v[160:161], v[160:161], v[208:209]\n\t"
    "ds_read_b64_tr_b16 v[82:83], v49 offset:2048\n\t"
    "ds_read_b64_tr_b16 v[84:85], v49 offset:2560\n\t"
    "v_mfma_f32_16x16x16_bf16 v[126:129], v[60:61], v[78:79], 0\n\t"
    "v_cvt_pk_bf16_f32 v66, v158, v159\n\t"
    "v_cvt_pk_bf16_f32 v67, v160, v161\n\t"
    "v_mfma_f32_16x16x16_bf16 v[150:153], v[60:61], v[80:81], 0\n\t"
    "v_pk_fma_f32 v[162:163], v[190:191], v[182:183], v[162:163] neg_lo:[1,0,0] neg_hi:[1,0,0]\n\t"
    "s_add_u32 m0, s63, 37888\n\t"
    "s_nop 0\n\t"
    "buffer_load_dwordx4 v42, s[24:27], s42 offen lds\n\t"
    "v_pk_fma_f32 v[164:165], v[192:193], v[182:183], v[164:165] neg_lo:[1,0,0] neg_hi:[1,0,0]\n\t"
    "ds_read_b64_tr_b16 v[114:115], v50 offset:2048\n\t"
    "ds_read_b64_tr_b16 v[116:117], v50 offset:2560\n\t"
    "v_mfma_f32_16x16x16_bf16 v[130:133], v[62:63], v[80:81], 0\n\t"
    "v_pk_mul_f32 v[162:163], v[162:163], v[210:211]\n\t"
    "v_pk_mul_f32 v[164:165], v[164:165], v[212:213]\n\t"
    "v_cvt_pk_bf16_f32 v68, v162, v163\n\t"
    "v_cvt_pk_bf16_f32 v69, v164, v165\n\t"
    "v_pk_fma_f32 v[166:167], v[194:195], v[182:183], v[166:167] neg_lo:[1,0,0] neg_hi:[1,0,0]\n\t"
    "v_pk_fma_f32 v[168:169], v[196:197], v[182:183], v[168:169] neg_lo:[1,0,0] neg_hi:[1,0,0]\n\t"
    "ds_read_b64_tr_b16 v[86:87], v49 offset:4096\n\t"
    "ds_read_b64_tr_b16 v[88:89], v49 offset:4608\n\t"
    "v_pk_mul_f32 v[166:167], v[166:167], v[214:215]\n\t"
    "v_pk_mul_f32 v[168:169], v[168:169], v[216:217]\n\t"
    "s_add_u32 m0, s63, 38912\n\t"
    "s_add_u32 s42, s80, 114688\n\t"
    "buffer_load_dwordx4 v42, s[20:23], s42 offen lds\n\t"
    "v_cvt_pk_bf16_f32 v70, v166, v167\n\t"
    "v_cvt_pk_bf16_f32 v71, v168, v169\n\t"
    "v_pk_fma_f32 v[170:171], v[198:199], v[182:183], v[170:171] neg_lo:[1,0,0] neg_hi:[1,0,0]\n\t"
    "v_pk_fma_f32 v[172:173], v[200:201], v[182:183], v[172:173] neg_lo:[1,0,0] neg_hi:[1,0,0]\n\t"
    "ds_read_b64_tr_b16 v[232:233], v50 offset:4096\n\t"
    "ds_read_b64_tr_b16 v[234:235], v50 offset:4608\n\t"
    "v_pk_mul_f32 v[170:171], v[170:171], v[218:219]\n\t"
    "v_pk_mul_f32 v[172:173], v[172:173], v[220:221]\n\t"
    "v_cvt_pk_bf16_f32 v72, v170, v171\n\t"
    "v_cvt_pk_bf16_f32 v73, v172, v173\n\t"
    "v_mov_b32 v10, 0xceabfb8f\n\t"
    "s_add_u32 m0, s63, 39936\n\t"
    "s_nop 0\n\t"
    "buffer_load_dwordx4 v42, s[24:27], s42 offen lds\n\t"
    "v_cndmask_b32_e64 v118, v118, v119, s[66:67]\n\t"
    "ds_read_b64_tr_b16 v[90:91], v49 offset:6144\n\t"
    "ds_read_b64_tr_b16 v[92:93], v49 offset:6656\n\t"
    "v_cndmask_b32_e64 v122, v122, v123, s[66:67]\n\t"
    "v_cndmask_b32_e64 v126, v126, v127, s[66:67]\n\t"
    "v_cndmask_b32_e64 v130, v130, v131, s[66:67]\n\t"
    "v_cndmask_b32_e64 v120, v120, v121, s[66:67]\n\t"
    "v_cndmask_b32_e64 v124, v124, v125, s[66:67]\n\t"
    "v_cndmask_b32_e64 v128, v128, v129, s[66:67]\n\t"
    "ds_read_b64_tr_b16 v[236:237], v50 offset:6144\n\t"
    "ds_read_b64_tr_b16 v[238:239], v50 offset:6656\n\t"
    "v_cndmask_b32_e64 v132, v132, v133, s[66:67]\n\t"
    "v_cndmask_b32_e64 v138, v138, v139, s[66:67]\n\t"
    "v_cndmask_b32_e64 v142, v142, v143, s[66:67]\n\t"
    "s_add_u32 m0, s63, 133120\n\t"
    "s_add_u32 s42, s80, 172032\n\t"
    "buffer_load_dwordx4 v42, s[20:23], s42 offen lds\n\t"
    "v_cndmask_b32_e64 v146, v146, v147, s[66:67]\n\t"
    "v_cndmask_b32_e64 v150, v150, v151, s[66:67]\n\t"
    "v_cndmask_b32_e64 v140, v140, v141, s[66:67]\n\t"
    "v_cndmask_b32_e64 v144, v144, v145, s[66:67]\n\t"
    "ds_read_b64_tr_b16 v[94:95], v49 offset:32768\n\t"
    "ds_read_b64_tr_b16 v[96:97], v49 offset:33280\n\t"
    "v_cndmask_b32_e64 v148, v148, v149, s[66:67]\n\t"
    "v_cndmask_b32_e64 v152, v152, v153, s[66:67]\n\t"
    "v_cndmask_b32_e64 v138, v138, v10, s[72:73]\n\t"
    "v_cndmask_b32_e64 v122, v142, v122, s[68:69]\n\t"
    "v_cndmask_b32_e64 v126, v146, v126, s[68:69]\n\t"
    "v_cndmask_b32_e64 v130, v150, v130, s[68:69]\n\t"
    "ds_read_b64_tr_b16 v[240:241], v50 offset:32768\n\t"
    "ds_read_b64_tr_b16 v[242:243], v50 offset:33280\n\t"
    "s_add_u32 m0, s63, 134144\n\t"
    "s_nop 0\n\t"
    "buffer_load_dwordx4 v42, s[24:27], s42 offen lds\n\t"
    "v_cndmask_b32_e64 v140, v140, v10, s[72:73]\n\t"
    "v_cndmask_b32_e64 v123, v144, v124, s[70:71]\n\t"
    "v_cndmask_b32_e64 v127, v148, v128, s[70:71]\n\t"
    "v_cndmask_b32_e64 v131, v152, v132, s[70:71]\n\t"
    "v_cndmask_b32_e64 v118, v138, v118, s[68:69]\n\t"
    "v_max_f32 v142, v122, v123\n\t"
    "ds_read_b64_tr_b16 v[98:99], v49 offset:34816\n\t"
    "ds_read_b64_tr_b16 v[100:101], v49 offset:35328\n\t"
    "v_max_f32 v146, v126, v127\n\t"
    "v_max_f32 v150, v130, v131\n\t"
    "v_cndmask_b32_e64 v119, v140, v120, s[70:71]\n\t"
    "s_add_u32 m0, s63, 135168\n\t"
    "s_add_u32 s42, s81, 0\n\t"
    "buffer_load_dwordx4 v42, s[20:23], s42 offen lds\n\t"
    "v_max_f32 v143, v122, v123\n\t"
    "v_max_f32 v147, v126, v127\n\t"
    "v_max_f32 v151, v130, v131\n\t"
    "ds_read_b64_tr_b16 v[244:245], v50 offset:34816\n\t"
    "ds_read_b64_tr_b16 v[246:247], v50 offset:35328\n\t"
    "v_max_f32 v138, v118, v119\n\t"
    "v_permlane16_swap_b32 v142, v143\n\t"
    "v_lshlrev_b32 v28, 6, v17\n\t"
    "v_permlane16_swap_b32 v146, v147\n\t"
    "v_permlane16_swap_b32 v150, v151\n\t"
    "v_max_f32 v139, v118, v119\n\t"
    "v_lshl_add_u32 v28, v18, 3, v28\n\t"
    "v_max_f32 v144, v142, v143\n\t"
    "v_max_f32 v148, v146, v147\n\t"
    "ds_read_b64_tr_b16 v[102:103], v49 offset:36864\n\t"
    "ds_read_b64_tr_b16 v[104:105], v49 offset:37376\n\t"
    "s_add_u32 m0, s63, 136192\n\t"
    "s_nop 0\n\t"
    "buffer_load_dwordx4 v42, s[24:27], s42 offen lds\n\t"
    "v_max_f32 v152, v150, v151\n\t"
    "v_add_u32 v28, s63, v28\n\t"
    "v_permlane16_swap_b32 v138, v139\n\t"
    "v_max_f32 v145, v142, v143\n\t"
    "v_max_f32 v149, v146, v147\n\t"
    "s_waitcnt vmcnt(18)\n\t"
    "ds_read_b64 v[20:21], v28 offset:0\n\t"
    "v_max_f32 v153, v150, v151\n\t"
    "v_max_f32 v140, v138, v139\n\t"
    "ds_read_b64_tr_b16 v[248:249], v50 offset:36864\n\t"
    "ds_read_b64_tr_b16 v[250:251], v50 offset:37376\n\t"
    "v_permlane32_swap_b32 v144, v145\n\t"
    "ds_read_b64 v[22:23], v28 offset:16\n\t"
    "v_permlane32_swap_b32 v148, v149\n\t"
    "v_permlane32_swap_b32 v152, v153\n\t"
    "v_max_f32 v141, v138, v139\n\t"
    "ds_read_b64 v[24:25], v28 offset:32\n\t"
    "s_add_u32 m0, s63, 137216\n\t"
    "s_add_u32 s42, s81, 57344\n\t"
    "buffer_load_dwordx4 v42, s[20:23], s42 offen lds\n\t"
    "v_max_f32 v142, v144, v145\n\t"
    "v_max_f32 v146, v148, v149\n\t"
    "ds_read_b64_tr_b16 v[106:107], v49 offset:38912\n\t"
    "ds_read_b64_tr_b16 v[108:109], v49 offset:39424\n\t"
    "v_max_f32 v150, v152, v153\n\t"
    "ds_read_b64 v[26:27], v28 offset:48\n\t"
    "v_permlane32_swap_b32 v140, v141\n\t"
    "v_sub_f32 v122, v122, v142\n\t"
    "v_sub_f32 v126, v126, v146\n\t"
    "v_sub_f32 v130, v130, v150\n\t"
    "v_max_f32 v138, v140, v141\n\t"
    "ds_read_b64_tr_b16 v[252:253], v50 offset:38912\n\t"
    "ds_read_b64_tr_b16 v[254:255], v50 offset:39424\n\t"
    "v_sub_f32 v123, v123, v142\n\t"
    "v_sub_f32 v127, v127, v146\n\t"
    "s_add_u32 m0, s63, 138240\n\t"
    "s_nop 0\n\t"
    "buffer_load_dwordx4 v42, s[24:27], s42 offen lds\n\t"
    "v_sub_f32 v131, v131, v150\n\t"
    "v_sub_f32 v118, v118, v138\n\t"
    "v_exp_f32 v122, v122\n\t"
    "v_exp_f32 v126, v126\n\t"
    "v_exp_f32 v130, v130\n\t"
    "v_sub_f32 v119, v119, v138\n\t"
    "v_exp_f32 v123, v123\n\t"
    "v_exp_f32 v127, v127\n\t"
    "v_exp_f32 v131, v131\n\t"
    "s_waitcnt lgkmcnt(0)\n\t"
    "s_add_u32 m0, s63, 65536\n\t"
    "s_add_u32 s42, s81, 114688\n\t"
    "buffer_load_dwordx4 v42, s[20:23], s42 offen lds\n\t"
    "v_exp_f32 v118, v118\n\t"
    "v_add_f32 v142, v122, v123\n\t"
    "v_add_f32 v146, v126, v127\n\t"
    "v_add_f32 v150, v130, v131\n\t"
    "v_exp_f32 v119, v119\n\t"
    "v_add_f32 v143, v122, v123\n\t"
    "v_add_f32 v147, v126, v127\n\t"
    "v_add_f32 v151, v130, v131\n\t"
    "v_add_f32 v138, v118, v119\n\t"
    "v_permlane16_swap_b32 v142, v143\n\t"
    "s_add_u32 m0, s63, 66560\n\t"
    "s_nop 0\n\t"
    "buffer_load_dwordx4 v42, s[24:27], s42 offen lds\n\t"
    "v_permlane16_swap_b32 v146, v147\n\t"
    "v_permlane16_swap_b32 v150, v151\n\t"
    "v_add_f32 v139, v118, v119\n\t"
    "v_add_f32 v144, v142, v143\n\t"
    "v_add_f32 v148, v146, v147\n\t"
    "v_add_f32 v152, v150, v151\n\t"
    "v_permlane16_swap_b32 v138, v139\n\t"
    "v_add_f32 v145, v142, v143\n\t"
    "v_add_f32 v149, v146, v147\n\t"
    "v_add_f32 v153, v150, v151\n\t"
    "s_add_u32 m0, s63, 67584\n\t"
    "s_add_u32 s42, s81, 172032\n\t"
    "buffer_load_dwordx4 v42, s[20:23], s42 offen lds\n\t"
    "v_add_f32 v140, v138, v139\n\t"
    "v_permlane32_swap_b32 v144, v145\n\t"
    "v_permlane32_swap_b32 v148, v149\n\t"
    "v_permlane32_swap_b32 v152, v153\n\t"
    "v_add_f32 v141, v138, v139\n\t"
    "v_add_f32 v142, v144, v145\n\t"
    "v_add_f32 v146, v148, v149\n\t"
    "v_add_f32 v150, v152, v153\n\t"
    "v_permlane32_swap_b32 v140, v141\n\t"
    "s_add_u32 m0, s63, 68608\n\t"
    "s_nop 0\n\t"
    "buffer_load_dwordx4 v42, s[24:27], s42 offen lds\n\t"
    "v_rcp_f32 v142, v142\n\t"
    "v_rcp_f32 v146, v146\n\t"
    "v_rcp_f32 v150, v150\n\t"
    "v_add_f32 v138, v140, v141\n\t"
    "v_pk_mul_f32 v[122:123], v[122:123], v[142:143] op_sel_hi:[1,0]\n\t"
    "v_pk_mul_f32 v[126:127], v[126:127], v[146:147] op_sel_hi:[1,0]\n\t"
    "v_pk_mul_f32 v[130:131], v[130:131], v[150:151] op_sel_hi:[1,0]\n\t"
    "v_rcp_f32 v138, v138\n\t"
    "v_cvt_pk_bf16_f32 v122, v122, v123\n\t"
    "v_cvt_pk_bf16_f32 v126, v126, v127\n\t"
    "s_add_u32 m0, s63, 69632\n\t"
    "s_add_u32 s42, s82, 0\n\t"
    "buffer_load_dwordx4 v42, s[20:23], s42 offen lds\n\t"
    "v_cvt_pk_bf16_f32 v130, v130, v131\n\t"
    "v_pk_mul_f32 v[118:119], v[118:119], v[138:139] op_sel_hi:[1,0]\n\t"
    "v_lshrrev_b32 v123, 16, v122\n\t"
    "v_lshrrev_b32 v127, 16, v126\n\t"
    "v_lshrrev_b32 v131, 16, v130\n\t"
    "v_cvt_pk_bf16_f32 v118, v118, v119\n\t"
    "v_and_b32 v122, 0xffff, v122\n\t"
    "v_and_b32 v126, 0xffff, v126\n\t"
    "v_and_b32 v130, 0xffff, v130\n\t"
    "v_lshrrev_b32 v119, 16, v118\n\t"
    "s_add_u32 m0, s63, 70656\n\t"
    "s_nop 0\n\t"
    "buffer_load_dwordx4 v42, s[24:27], s42 offen lds\n\t"
    "v_lshlrev_b32 v122, v40, v122\n\t"
    "v_lshlrev_b32 v126, v40, v126\n\t"
    "v_lshlrev_b32 v130, v40, v130\n\t"
    "v_and_b32 v118, 0xffff, v118\n\t"
    "v_lshlrev_b32 v123, v40, v123\n\t"
    "v_lshlrev_b32 v127, v40, v127\n\t"
    "v_lshlrev_b32 v131, v40, v131\n\t"
    "v_lshlrev_b32 v118, v40, v118\n\t"
    "v_cndmask_b32_e64 v142, v122, 0, s[68:69]\n\t"
    "v_cndmask_b32_e64 v146, v126, 0, s[68:69]\n\t"
    "v_cndmask_b32_e64 v150, v130, 0, s[68:69]\n\t"
    "v_lshlrev_b32 v119, v40, v119\n\t"
    "v_cndmask_b32_e64 v143, v123, 0, s[70:71]\n\t"
    "v_cndmask_b32_e64 v147, v127, 0, s[70:71]\n\t"
    "v_cndmask_b32_e64 v151, v131, 0, s[70:71]\n\t"
    "v_cndmask_b32_e64 v138, v118, 0, s[68:69]\n\t"
    "v_cndmask_b32_e64 v144, 0, v122, s[68:69]\n\t"
    "v_cndmask_b32_e64 v148, 0, v126, s[68:69]\n\t"
    "v_cndmask_b32_e64 v152, 0, v130, s[68:69]\n\t"
    "v_cndmask_b32_e64 v139, v119, 0, s[70:71]\n\t"
    "v_cndmask_b32_e64 v145, 0, v123, s[70:71]\n\t"
    "v_cndmask_b32_e64 v149, 0, v127, s[70:71]\n\t"
    "v_cndmask_b32_e64 v153, 0, v131, s[70:71]\n\t"
    "v_cndmask_b32_e64 v140, 0, v118, s[68:69]\n\t"
    "v_mfma_f32_16x16x32_bf16 v[122:125], v[66:69], v[142:145], 0\n\t"
    "v_mfma_f32_16x16x32_bf16 v[126:129], v[68:71], v[146:149], 0\n\t"
    "v_mfma_f32_16x16x32_bf16 v[130:133], v[70:73], v[150:153], 0\n\t"
    "v_cndmask_b32_e64 v141, 0, v119, s[70:71]\n\t"
    "s_nop 1\n\t"
    "v_mfma_f32_16x16x32_bf16 v[118:121], v[64:67], v[138:141], 0\n\t"
    "v_readfirstlane_b32 s38, v16\n\t"
    "s_nop 1\n\t"
    "s_mul_i32 s37, s38, 0x9E3779B1\n\t"
    "s_add_u32 s37, s37, 0x7F4A7C15\n\t"
    "s_lshl_b32 s39, s47, 16\n\t"
    "s_lshl2_add_u32 s40, s46, s33\n\t"
    "s_lshl_b32 s40, s40, 10\n\t"
    "s_add_u32 s39, s39, s40\n\t"
    "s_add_u32 s39, s39, 1048576\n\t"
    "s_add_u32 s40, s39, 32768\n\t"
    "v_cvt_pk_bf16_f32 v138, v118, v119\n\t"
    "v_cvt_pk_bf16_f32 v139, v120, v121\n\t"
    "v_cvt_pk_bf16_f32 v140, v122, v123\n\t"
    "v_cvt_pk_bf16_f32 v141, v124, v125\n\t"
    "v_cvt_pk_bf16_f32 v146, v126, v127\n\t"
    "v_cvt_pk_bf16_f32 v147, v128, v129\n\t"
    "v_cvt_pk_bf16_f32 v148, v130, v131\n\t"
    "v_cvt_pk_bf16_f32 v149, v132, v133\n\t"
    "v_permlane16_swap_b32 v138, v140\n\t"
    "v_permlane16_swap_b32 v139, v141\n\t"
    "buffer_store_dwordx4 v[138:141], v19, s[28:31], s39 offen sc1\n\t"
    "v_permlane16_swap_b32 v146, v148\n\t"
    "v_permlane16_swap_b32 v147, v149\n\t"
    "buffer_store_dwordx4 v[146:149], v19, s[28:31], s40 offen sc1\n\t"
    "v_lshrrev_b32 v7, 2, v11\n\t"
    "v_xor_b32 v7, v7, v12\n\t"
    "v_and_b32 v7, 1, v7\n\t"
    "v_cmp_eq_u32 vcc, 0, v7\n\t"
    "s_nop 1\n\t"
    "v_cndmask_b32 v60, 0, v36, vcc\n\t"
    "v_cndmask_b32 v61, 0, v36, vcc\n\t"
    "v_cndmask_b32 v62, 0, v36, vcc\n\t"
    "v_cndmask_b32 v63, 0, v36, vcc\n\t"
    "v_mov_b32 v2, s37\n\t"
    "s_lshl3_add_u32 s48, s47, s46\n\t"
    "s_mul_i32 s48, s48, 128\n\t"
    "s_add_u32 s48, s48, 65536\n\t"
    "s_add_u32 m0, s63, 71680\n\t"
    "s_add_u32 s42, s82, 57344\n\t"
    "buffer_load_dwordx4 v42, s[20:23], s42 offen lds\n\t"
    "s_add_u32 m0, s63, 72704\n\t"
    "s_nop 0\n\t"
    "buffer_load_dwordx4 v42, s[24:27], s42 offen lds\n\t"
    "s_add_u32 m0, s63, 98304\n\t"
    "s_add_u32 s42, s82, 114688\n\t"
    "buffer_load_dwordx4 v42, s[20:23], s42 offen lds\n\t"
    "s_add_u32 m0, s63, 99328\n\t"
    "s_nop 0\n\t"
    "buffer_load_dwordx4 v42, s[24:27], s42 offen lds\n\t"
    "s_waitcnt vmcnt(30)\n\t"
    "ds_read_b64_tr_b16 a[0:1], v46 offset:2048\n\t"
    "ds_read_b64_tr_b16 a[2:3], v46 offset:2560\n\t"
    "ds_read_b64_tr_b16 a[4:5], v46 offset:3072\n\t"
    "ds_read_b64_tr_b16 a[6:7], v46 offset:3584\n\t"
    "s_waitcnt vmcnt(28)\n\t"
    "ds_read_b64_tr_b16 a[8:9], v46 offset:4096\n\t"
    "ds_read_b64_tr_b16 a[10:11], v46 offset:4608\n\t"
    "ds_read_b64_tr_b16 a[12:13], v46 offset:5120\n\t"
    "ds_read_b64_tr_b16 a[14:15], v46 offset:5632\n\t"
    "s_waitcnt lgkmcnt(6)\n\t"
    "v_mfma_f32_16x16x32_bf16 v[52:55], v[60:63], a[0:3], 0\n\t"
    "s_waitcnt vmcnt(26)\n\t"
    "ds_read_b64_tr_b16 a[16:17], v46 offset:6144\n\t"
    "ds_read_b64_tr_b16 a[18:19], v46 offset:6656\n\t"
    "s_waitcnt lgkmcnt(6)\n\t"
    "v_mfma_f32_16x16x32_bf16 v[56:59], v[60:63], a[4:7], 0\n\t"
    "ds_read_b64_tr_b16 a[20:21], v46 offset:7168\n\t"
    "ds_read_b64_tr_b16 a[22:23], v46 offset:7680\n\t"
    "s_waitcnt lgkmcnt(6)\n\t"
    "v_mfma_f32_16x16x32_bf16 v[52:55], v[60:63], a[8:11], v[52:55]\n\t"
    "s_waitcnt vmcnt(24)\n\t"
    "ds_read_b64_tr_b16 a[24:25], v46 offset:32768\n\t"
    "ds_read_b64_tr_b16 a[26:27], v46 offset:33280\n\t"
    "s_waitcnt lgkmcnt(6)\n\t"
    "v_mfma_f32_16x16x32_bf16 v[56:59], v[60:63], a[12:15], v[56:59]\n\t"
    "ds_read_b64_tr_b16 a[28:29], v46 offset:33792\n\t"
    "ds_read_b64_tr_b16 a[30:31], v46 offset:34304\n\t"
    "s_waitcnt lgkmcnt(6)\n\t"
    "v_mfma_f32_16x16x32_bf16 v[52:55], v[60:63], a[16:19], v[52:55]\n\t"
    "s_waitcnt vmcnt(22)\n\t"
    "ds_read_b64_tr_b16 a[32:33], v46 offset:34816\n\t"
    "ds_read_b64_tr_b16 a[34:35], v46 offset:35328\n\t"
    "s_waitcnt lgkmcnt(6)\n\t"
    "v_mfma_f32_16x16x32_bf16 v[56:59], v[60:63], a[20:23], v[56:59]\n\t"
    "ds_read_b64_tr_b16 a[36:37], v46 offset:35840\n\t"
    "ds_read_b64_tr_b16 a[38:39], v46 offset:36352\n\t"
    "s_waitcnt lgkmcnt(6)\n\t"
    "v_mfma_f32_16x16x32_bf16 v[52:55], v[60:63], a[24:27], v[52:55]\n\t"
    "s_waitcnt vmcnt(20)\n\t"
    "ds_read_b64_tr_b16 a[40:41], v46 offset:36864\n\t"
    "ds_read_b64_tr_b16 a[42:43], v46 offset:37376\n\t"
    "s_waitcnt lgkmcnt(6)\n\t"
    "v_mfma_f32_16x16x32_bf16 v[56:59], v[60:63], a[28:31], v[56:59]\n\t"
    "ds_read_b64_tr_b16 a[44:45], v46 offset:37888\n\t"
    "ds_read_b64_tr_b16 a[46:47], v46 offset:38400\n\t"
    "s_waitcnt lgkmcnt(6)\n\t"
    "v_mfma_f32_16x16x32_bf16 v[52:55], v[60:63], a[32:35], v[52:55]\n\t"
    "s_waitcnt lgkmcnt(4)\n\t"
    "v_mfma_f32_16x16x32_bf16 v[56:59], v[60:63], a[36:39], v[56:59]\n\t"
    "s_waitcnt lgkmcnt(2)\n\t"
    "v_mfma_f32_16x16x32_bf16 v[52:55], v[60:63], a[40:43], v[52:55]\n\t"
    "s_waitcnt lgkmcnt(0)\n\t"
    "v_mfma_f32_16x16x32_bf16 v[56:59], v[60:63], a[44:47], v[56:59]\n\t"
    "s_waitcnt vmcnt(4)\n\t"
    "s_barrier\n\t"
    "s_cmp_lg_u32 s33, 1\n\t"
    "s_cbranch_scc1 L_pub1_%=\n\t"
    "s_mov_b64 exec, 1\n\t"
    "buffer_store_dword v2, off, s[28:31], s48 sc1\n\t"
    "s_mov_b64 exec, -1\n\t"
    "L_pub1_%=:\n\t"
    "s_lshl2_add_u32 s48, s64, s33\n\t"
    "s_mul_i32 s48, s48, 256\n\t"
    "s_add_u32 s48, s48, 65536\n\t"
    "v_min_u32 v3, 1, v1\n\t"
    "v_mul_u32_u24 v3, 128, v3\n\t"
    "s_add_u32 m0, s63, 100352\n\t"
    "s_add_u32 s42, s82, 172032\n\t"
    "buffer_load_dwordx4 v42, s[20:23], s42 offen lds\n\t"
    "s_add_u32 m0, s63, 101376\n\t"
    "s_nop 0\n\t"
    "buffer_load_dwordx4 v42, s[24:27], s42 offen lds\n\t"
    "s_add_u32 m0, s63, 102400\n\t"
    "s_add_u32 s42, s83, 0\n\t"
    "buffer_load_dwordx4 v42, s[20:23], s42 offen lds\n\t"
    "s_add_u32 m0, s63, 103424\n\t"
    "s_nop 0\n\t"
    "buffer_load_dwordx4 v42, s[24:27], s42 offen lds\n\t"
    "s_add_u32 m0, s63, 104448\n\t"
    "s_add_u32 s42, s83, 57344\n\t"
    "buffer_load_dwordx4 v42, s[20:23], s42 offen lds\n\t"
    "s_add_u32 m0, s63, 105472\n\t"
    "s_nop 0\n\t"
    "buffer_load_dwordx4 v42, s[24:27], s42 offen lds\n\t"
    "s_add_u32 m0, s63, 0\n\t"
    "s_add_u32 s42, s83, 114688\n\t"
    "buffer_load_dwordx4 v42, s[20:23], s42 offen lds\n\t"
    "s_add_u32 m0, s63, 1024\n\t"
    "s_nop 0\n\t"
    "buffer_load_dwordx4 v42, s[24:27], s42 offen lds\n\t"
    "s_add_u32 m0, s63, 2048\n\t"
    "s_add_u32 s42, s83, 172032\n\t"
    "buffer_load_dwordx4 v42, s[20:23], s42 offen lds\n\t"
    "s_add_u32 m0, s63, 3072\n\t"
    "s_nop 0\n\t"
    "buffer_load_dwordx4 v42, s[24:27], s42 offen lds\n\t"
    "s_lshr_b32 s39, s33, 1\n\t"
    "s_lshl1_add_u32 s39, s64, s39\n\t"
    "s_lshl_b32 s44, s39, 15\n\t"
    "s_add_u32 s39, s44, s63\n\t"
    "v_add_u32 v5, s39, v19\n\t"
    "v_xor_b32 v6, 32768, v5\n\t"
    "s_add_u32 s39, s87, 0\n\t"
    "s_and_b32 s39, s39, 7\n\t"
    "s_lshl_b32 s39, s39, 10\n\t"
    "s_add_u32 s45, s39, 1048576\n\t"
    "s_add_u32 s39, s87, 1\n\t"
    "s_and_b32 s39, s39, 7\n\t"
    "s_lshl_b32 s39, s39, 10\n\t"
    "s_add_u32 s46, s39, 1048576\n\t"
    "s_add_u32 s39, s87, 2\n\t"
    "s_and_b32 s39, s39, 7\n\t"
    "s_lshl_b32 s39, s39, 10\n\t"
    "s_add_u32 s47, s39, 1048576\n\t"
    "s_add_u32 s39, s87, 3\n\t"
    "s_and_b32 s39, s39, 7\n\t"
    "s_lshl_b32 s39, s39, 10\n\t"
    "s_add_u32 s49, s39, 1048576\n\t"
    "s_add_u32 s39, s87, 4\n\t"
    "s_and_b32 s39, s39, 7\n\t"
    "s_lshl_b32 s39, s39, 10\n\t"
    "s_add_u32 s50, s39, 1048576\n\t"
    "s_add_u32 s39, s87, 5\n\t"
    "s_and_b32 s39, s39, 7\n\t"
    "s_lshl_b32 s39, s39, 10\n\t"
    "s_add_u32 s51, s39, 1048576\n\t"
    "s_add_u32 s39, s87, 6\n\t"
    "s_and_b32 s39, s39, 7\n\t"
    "s_lshl_b32 s39, s39, 10\n\t"
    "s_add_u32 s52, s39, 1048576\n\t"
    "s_add_u32 s39, s87, 7\n\t"
    "s_and_b32 s39, s39, 7\n\t"
    "s_lshl_b32 s39, s39, 10\n\t"
    "s_add_u32 s53, s39, 1048576\n\t"
    "v_add_u32 v13, s63, v19\n\t"
    "v_xor_b32 v14, 8192, v13\n\t"
    "v_xor_b32 v16, 16384, v13\n\t"
    "v_xor_b32 v28, 24576, v13\n\t"
    "ds_read_b64_tr_b16 a[48:49], v46 offset:38912\n\t"
    "ds_read_b64_tr_b16 a[50:51], v46 offset:39424\n\t"
    "ds_read_b64_tr_b16 a[52:53], v46 offset:39936\n\t"
    "ds_read_b64_tr_b16 a[54:55], v46 offset:40448\n\t"
    "ds_read_b64_tr_b16 a[56:57], v48 offset:2048\n\t"
    "ds_read_b64_tr_b16 a[58:59], v48 offset:2560\n\t"
    "ds_read_b64_tr_b16 a[60:61], v48 offset:3072\n\t"
    "ds_read_b64_tr_b16 a[62:63], v48 offset:3584\n\t"
    "s_waitcnt lgkmcnt(6)\n\t"
    "v_mfma_f32_16x16x32_bf16 v[52:55], v[60:63], a[48:51], v[52:55]\n\t"
    "ds_read_b64_tr_b16 a[64:65], v48 offset:4096\n\t"
    "ds_read_b64_tr_b16 a[66:67], v48 offset:4608\n\t"
    "s_waitcnt lgkmcnt(6)\n\t"
    "v_mfma_f32_16x16x32_bf16 v[56:59], v[60:63], a[52:55], v[56:59]\n\t"
    "ds_read_b64_tr_b16 a[68:69], v48 offset:5120\n\t"
    "ds_read_b64_tr_b16 a[70:71], v48 offset:5632\n\t"
    "s_waitcnt lgkmcnt(6)\n\t"
    "v_mfma_f32_16x16x32_bf16 v[52:55], v[60:63], a[56:59], v[52:55]\n\t"
    "ds_read_b64_tr_b16 a[72:73], v48 offset:6144\n\t"
    "ds_read_b64_tr_b16 a[74:75], v48 offset:6656\n\t"
    "s_waitcnt lgkmcnt(6)\n\t"
    "v_mfma_f32_16x16x32_bf16 v[56:59], v[60:63], a[60:63], v[56:59]\n\t"
    "ds_read_b64_tr_b16 a[76:77], v48 offset:7168\n\t"
    "ds_read_b64_tr_b16 a[78:79], v48 offset:7680\n\t"
    "s_waitcnt lgkmcnt(6)\n\t"
    "v_mfma_f32_16x16x32_bf16 v[52:55], v[60:63], a[64:67], v[52:55]\n\t"
    "ds_read_b64_tr_b16 a[80:81], v47 offset:0\n\t"
    "ds_read_b64_tr_b16 a[82:83], v47 offset:512\n\t"
    "s_waitcnt lgkmcnt(6)\n\t"
    "v_mfma_f32_16x16x32_bf16 v[56:59], v[60:63], a[68:71], v[56:59]\n\t"
    "ds_read_b64_tr_b16 a[84:85], v47 offset:1024\n\t"
    "ds_read_b64_tr_b16 a[86:87], v47 offset:1536\n\t"
    "s_waitcnt lgkmcnt(6)\n\t"
    "v_mfma_f32_16x16x32_bf16 v[52:55], v[60:63], a[72:75], v[52:55]\n\t"
    "ds_read_b64_tr_b16 a[88:89], v47 offset:2048\n\t"
    "ds_read_b64_tr_b16 a[90:91], v47 offset:2560\n\t"
    "s_waitcnt lgkmcnt(6)\n\t"
    "v_mfma_f32_16x16x32_bf16 v[56:59], v[60:63], a[76:79], v[56:59]\n\t"
    "ds_read_b64_tr_b16 a[92:93], v47 offset:3072\n\t"
    "ds_read_b64_tr_b16 a[94:95], v47 offset:3584\n\t"
    "ds_read_b64_tr_b16 a[96:97], v47 offset:4096\n\t"
    "ds_read_b64_tr_b16 a[98:99], v47 offset:4608\n\t"
    "ds_read_b64_tr_b16 a[100:101], v47 offset:5120\n\t"
    "ds_read_b64_tr_b16 a[102:103], v47 offset:5632\n\t"
    "buffer_load_dword v4, v3, s[28:31], s48 offen sc1\n\t"
    "s_waitcnt lgkmcnt(10)\n\t"
    "v_mfma_f32_16x16x32_bf16 v[52:55], v[60:63], a[80:83], v[52:55]\n\t"
    "s_waitcnt lgkmcnt(8)\n\t"
    "v_mfma_f32_16x16x32_bf16 v[56:59], v[60:63], a[84:87], v[56:59]\n\t"
    "s_waitcnt lgkmcnt(6)\n\t"
    "v_mfma_f32_16x16x32_bf16 v[52:55], v[60:63], a[88:91], v[52:55]\n\t"
    "s_waitcnt lgkmcnt(4)\n\t"
    "v_mfma_f32_16x16x32_bf16 v[56:59], v[60:63], a[92:95], v[56:59]\n\t"
    "s_waitcnt lgkmcnt(2)\n\t"
    "v_mfma_f32_16x16x32_bf16 v[52:55], v[60:63], a[96:99], v[52:55]\n\t"
    "s_waitcnt lgkmcnt(0)\n\t"
    "v_mfma_f32_16x16x32_bf16 v[56:59], v[60:63], a[100:103], v[56:59]\n\t"
    "s_waitcnt vmcnt(0)\n\t"
    "v_cmp_ne_u32 vcc, v4, v2\n\t"
    "s_mov_b32 s41, 40000\n\t"
    "s_nop 3\n\t"
    "s_cbranch_vccz L_qe2_%=\n\t"
    "L_ql3_%=:\n\t"
    "buffer_load_dword v4, v3, s[28:31], s48 offen sc1\n\t"
    "s_sub_u32 s41, s41, 1\n\t"
    "s_cbranch_scc1 L_qe2_%=\n\t"
    "s_waitcnt vmcnt(0)\n\t"
    "v_cmp_ne_u32 vcc, v4, v2\n\t"
    "s_nop 4\n\t"
    "s_cbranch_vccnz L_ql3_%=\n\t"
    "L_qe2_%=:\n\t"
    "buffer_load_dwordx4 a[224:227], v6, s[28:31], s45 offen sc1\n\t"
    "buffer_load_dwordx4 a[228:231], v6, s[28:31], s46 offen sc1\n\t"
    "buffer_load_dwordx4 a[232:235], v6, s[28:31], s47 offen sc1\n\t"
    "buffer_load_dwordx4 a[236:239], v6, s[28:31], s49 offen sc1\n\t"
    "buffer_load_dwordx4 a[240:243], v6, s[28:31], s50 offen sc1\n\t"
    "buffer_load_dwordx4 a[244:247], v6, s[28:31], s51 offen sc1\n\t"
    "buffer_load_dwordx4 a[248:251], v6, s[28:31], s52 offen sc1\n\t"
    "buffer_load_dwordx4 a[252:255], v6, s[28:31], s53 offen sc1\n\t"
    "buffer_load_dwordx4 a[192:195], v5, s[28:31], s45 offen sc1\n\t"
    "buffer_load_dwordx4 a[196:199], v5, s[28:31], s46 offen sc1\n\t"
    "buffer_load_dwordx4 a[200:203], v5, s[28:31], s47 offen sc1\n\t"
    "buffer_load_dwordx4 a[204:207], v5, s[28:31], s49 offen sc1\n\t"
    "buffer_load_dwordx4 a[208:211], v5, s[28:31], s50 offen sc1\n\t"
    "buffer_load_dwordx4 a[212:215], v5, s[28:31], s51 offen sc1\n\t"
    "buffer_load_dwordx4 a[216:219], v5, s[28:31], s52 offen sc1\n\t"
    "buffer_load_dwordx4 a[220:223], v5, s[28:31], s53 offen sc1\n\t"
    "ds_read_b64_tr_b16 a[104:105], v47 offset:6144\n\t"
    "ds_read_b64_tr_b16 a[106:107], v47 offset:6656\n\t"
    "ds_read_b64_tr_b16 a[108:109], v47 offset:7168\n\t"
    "ds_read_b64_tr_b16 a[110:111], v47 offset:7680\n\t"
    "ds_read_b64_tr_b16 a[112:113], v47 offset:32768\n\t"
    "ds_read_b64_tr_b16 a[114:115], v47 offset:33280\n\t"
    "ds_read_b64_tr_b16 a[116:117], v47 offset:33792\n\t"
    "ds_read_b64_tr_b16 a[118:119], v47 offset:34304\n\t"
    "s_waitcnt lgkmcnt(6)\n\t"
    "v_mfma_f32_16x16x32_bf16 v[52:55], v[60:63], a[104:107], v[52:55]\n\t"
    "ds_read_b64_tr_b16 a[120:121], v47 offset:34816\n\t"
    "ds_read_b64_tr_b16 a[122:123], v47 offset:35328\n\t"
    "s_waitcnt lgkmcnt(6)\n\t"
    "v_mfma_f32_16x16x32_bf16 v[56:59], v[60:63], a[108:111], v[56:59]\n\t"
    "ds_read_b64_tr_b16 a[124:125], v47 offset:35840\n\t"
    "ds_read_b64_tr_b16 a[126:127], v47 offset:36352\n\t"
    "s_waitcnt lgkmcnt(6)\n\t"
    "v_mfma_f32_16x16x32_bf16 v[52:55], v[60:63], a[112:115], v[52:55]\n\t"
    "ds_read_b64_tr_b16 a[128:129], v47 offset:36864\n\t"
    "ds_read_b64_tr_b16 a[130:131], v47 offset:37376\n\t"
    "s_waitcnt lgkmcnt(6)\n\t"
    "v_mfma_f32_16x16x32_bf16 v[56:59], v[60:63], a[116:119], v[56:59]\n\t"
    "ds_read_b64_tr_b16 a[132:133], v47 offset:37888\n\t"
    "ds_read_b64_tr_b16 a[134:135], v47 offset:38400\n\t"
    "s_waitcnt lgkmcnt(6)\n\t"
    "v_mfma_f32_16x16x32_bf16 v[52:55], v[60:63], a[120:123], v[52:55]\n\t"
    "ds_read_b64_tr_b16 a[136:137], v47 offset:38912\n\t"
    "ds_read_b64_tr_b16 a[138:139], v47 offset:39424\n\t"
    "s_waitcnt lgkmcnt(6)\n\t"
    "v_mfma_f32_16x16x32_bf16 v[56:59], v[60:63], a[124:127], v[56:59]\n\t"
    "ds_read_b64_tr_b16 a[140:141], v47 offset:39936\n\t"
    "ds_read_b64_tr_b16 a[142:143], v47 offset:40448\n\t"
    "s_waitcnt lgkmcnt(6)\n\t"
    "v_mfma_f32_16x16x32_bf16 v[52:55], v[60:63], a[128:131], v[52:55]\n\t"
    "ds_read_b64_tr_b16 a[144:145], v46 offset:0\n\t"
    "ds_read_b64_tr_b16 a[146:147], v46 offset:512\n\t"
    "s_waitcnt lgkmcnt(6)\n\t"
    "v_mfma_f32_16x16x32_bf16 v[56:59], v[60:63], a[132:135], v[56:59]\n\t"
    "ds_read_b64_tr_b16 a[148:149], v46 offset:1024\n\t"
    "ds_read_b64_tr_b16 a[150:151], v46 offset:1536\n\t"
    "s_waitcnt lgkmcnt(6)\n\t"
    "v_mfma_f32_16x16x32_bf16 v[52:55], v[60:63], a[136:139], v[52:55]\n\t"
    "ds_read_b64_tr_b16 a[152:153], v46 offset:2048\n\t"
    "ds_read_b64_tr_b16 a[154:155], v46 offset:2560\n\t"
    "s_waitcnt lgkmcnt(6)\n\t"
    "v_mfma_f32_16x16x32_bf16 v[56:59], v[60:63], a[140:143], v[56:59]\n\t"
    "ds_read_b64_tr_b16 a[156:157], v46 offset:3072\n\t"
    "ds_read_b64_tr_b16 a[158:159], v46 offset:3584\n\t"
    "s_waitcnt lgkmcnt(6)\n\t"
    "v_mfma_f32_16x16x32_bf16 v[52:55], v[60:63], a[144:147], v[52:55]\n\t"
    "s_waitcnt lgkmcnt(4)\n\t"
    "v_mfma_f32_16x16x32_bf16 v[56:59], v[60:63], a[148:151], v[56:59]\n\t"
    "s_waitcnt lgkmcnt(2)\n\t"
    "v_mfma_f32_16x16x32_bf16 v[52:55], v[60:63], a[152:155], v[52:55]\n\t"
    "s_waitcnt lgkmcnt(0)\n\t"
    "v_mfma_f32_16x16x32_bf16 v[56:59], v[60:63], a[156:159], v[56:59]\n\t"
    "v_lshlrev_b32 v64, 16, v20\n\t"
    "v_and_b32 v65, 0xffff0000, v20\n\t"
    "v_lshlrev_b32 v66, 16, v21\n\t"
    "v_and_b32 v67, 0xffff0000, v21\n\t"
    "v_lshlrev_b32 v68, 16, v22\n\t"
    "v_and_b32 v69, 0xffff0000, v22\n\t"
    "v_lshlrev_b32 v70, 16, v23\n\t"
    "v_and_b32 v71, 0xffff0000, v23\n\t"
    "v_lshlrev_b32 v72, 16, v24\n\t"
    "v_and_b32 v73, 0xffff0000, v24\n\t"
    "v_lshlrev_b32 v74, 16, v25\n\t"
    "v_and_b32 v75, 0xffff0000, v25\n\t"
    "v_lshlrev_b32 v76, 16, v26\n\t"
    "v_and_b32 v77, 0xffff0000, v26\n\t"
    "v_lshlrev_b32 v78, 16, v27\n\t"
    "v_and_b32 v79, 0xffff0000, v27\n\t"
    "s_waitcnt vmcnt(15)\n\t"
    "v_mfma_f32_32x32x16_bf16 v[150:165], v[110:113], a[224:227], 0\n\t"
    "s_add_u32 m0, s63, 131072\n\t"
    "s_add_u32 s42, s84, 0\n\t"
    "buffer_load_dwordx4 v42, s[20:23], s42 offen lds\n\t"
    "v_mfma_f32_32x32x16_bf16 v[134:149], v[30:33], a[224:227], 0\n\t"
    "s_waitcnt vmcnt(15)\n\t"
    "v_mfma_f32_32x32x16_bf16 v[150:165], v[114:117], a[228:231], v[150:165]\n\t"
    "v_mfma_f32_32x32x16_bf16 v[134:149], v[82:85], a[228:231], v[134:149]\n\t"
    "s_waitcnt vmcnt(14)\n\t"
    "v_mfma_f32_32x32x16_bf16 v[150:165], v[232:235], a[232:235], v[150:165]\n\t"
    "s_add_u32 m0, s63, 132096\n\t"
    "s_nop 0\n\t"
    "buffer_load_dwordx4 v42, s[24:27], s42 offen lds\n\t"
    "v_mfma_f32_32x32x16_bf16 v[134:149], v[86:89], a[232:235], v[134:149]\n\t"
    "s_waitcnt vmcnt(14)\n\t"
    "v_mfma_f32_32x32x16_bf16 v[150:165], v[236:239], a[236:239], v[150:165]\n\t"
    "v_mfma_f32_32x32x16_bf16 v[134:149], v[90:93], a[236:239], v[134:149]\n\t"
    "s_waitcnt vmcnt(13)\n\t"
    "v_mfma_f32_32x32x16_bf16 v[150:165], v[240:243], a[240:243], v[150:165]\n\t"
    "s_add_u32 m0, s63, 133120\n\t"
    "s_add_u32 s42, s84, 57344\n\t"
    "buffer_load_dwordx4 v42, s[20:23], s42 offen lds\n\t"
    "v_mfma_f32_32x32x16_bf16 v[134:149], v[94:97], a[240:243], v[134:149]\n\t"
    "s_waitcnt vmcnt(13)\n\t"
    "v_mfma_f32_32x32x16_bf16 v[150:165], v[244:247], a[244:247], v[150:165]\n\t"
    "v_mfma_f32_32x32x16_bf16 v[134:149], v[98:101], a[244:247], v[134:149]\n\t"
    "s_waitcnt vmcnt(12)\n\t"
    "v_mfma_f32_32x32x16_bf16 v[150:165], v[248:251], a[248:251], v[150:165]\n\t"
    "s_add_u32 m0, s63, 134144\n\t"
    "s_nop 0\n\t"
    "buffer_load_dwordx4 v42, s[24:27], s42 offen lds\n\t"
    "v_mfma_f32_32x32x16_bf16 v[134:149], v[102:105], a[248:251], v[134:149]\n\t"
    "s_waitcnt vmcnt(12)\n\t"
    "v_mfma_f32_32x32x16_bf16 v[150:165], v[252:255], a[252:255], v[150:165]\n\t"
    "v_mfma_f32_32x32x16_bf16 v[134:149], v[106:109], a[252:255], v[134:149]\n\t"
    "s_waitcnt vmcnt(11)\n\t"
    "v_mfma_f32_32x32x16_bf16 v[118:133], v[110:113], a[192:195], 0\n\t"
    "s_waitcnt vmcnt(10)\n\t"
    "v_mfma_f32_32x32x16_bf16 v[118:133], v[114:117], a[196:199], v[118:133]\n\t"
    "s_nop 6\n\t"
    "ds_write_b128 v13, v[150:153] offset:36864\n\t"
    "s_waitcnt vmcnt(9)\n\t"
    "v_mfma_f32_32x32x16_bf16 v[118:133], v[232:235], a[200:203], v[118:133]\n\t"
    "ds_write_b128 v13, v[154:157] offset:37888\n\t"
    "s_waitcnt vmcnt(8)\n\t"
    "v_mfma_f32_32x32x16_bf16 v[118:133], v[236:239], a[204:207], v[118:133]\n\t"
    "ds_write_b128 v13, v[158:161] offset:38912\n\t"
    "s_waitcnt vmcnt(7)\n\t"
    "v_mfma_f32_32x32x16_bf16 v[118:133], v[240:243], a[208:211], v[118:133]\n\t"
    "ds_write_b128 v13, v[162:165] offset:39936\n\t"
    "s_waitcnt vmcnt(6)\n\t"
    "v_mfma_f32_32x32x16_bf16 v[118:133], v[244:247], a[212:215], v[118:133]\n\t"
    "s_waitcnt vmcnt(5)\n\t"
    "v_mfma_f32_32x32x16_bf16 v[118:133], v[248:251], a[216:219], v[118:133]\n\t"
    "ds_write_b128 v13, v[134:137] offset:32768\n\t"
    "s_waitcnt vmcnt(4)\n\t"
    "v_mfma_f32_32x32x16_bf16 v[118:133], v[252:255], a[220:223], v[118:133]\n\t"
    "ds_write_b128 v13, v[138:141] offset:33792\n\t"
    "s_and_b32 s40, s33, 1\n\t"
    "s_lshl1_add_u32 s40, s65, s40\n\t"
    "s_lshl_b32 s41, s40, 11\n\t"
    "v_mfma_f32_32x32x16_bf16 v[64:79], v[30:33], a[192:195], v[64:79]\n\t"
    "ds_write_b128 v13, v[142:145] offset:34816\n\t"
    "s_add_u32 s39, s44, s41\n\t"
    "s_add_u32 s39, s39, 3145728\n\t"
    "v_mfma_f32_32x32x16_bf16 v[64:79], v[82:85], a[196:199], v[64:79]\n\t"
    "ds_write_b128 v13, v[146:149] offset:35840\n\t"
    "s_lshr_b32 s44, s44, 3\n\t"
    "s_lshl_b32 s40, s40, 8\n\t"
    "s_add_u32 s44, s44, s40\n\t"
    "v_mfma_f32_32x32x16_bf16 v[64:79], v[86:89], a[200:203], v[64:79]\n\t"
    "s_add_u32 s44, s44, 524288\n\t"
    "v_lshlrev_b32 v9, 2, v1\n\t"
    "v_mfma_f32_32x32x16_bf16 v[64:79], v[90:93], a[204:207], v[64:79]\n\t"
    "ds_write_b128 v13, v[118:121] offset:4096\n\t"
    "v_mfma_f32_32x32x16_bf16 v[64:79], v[94:97], a[208:211], v[64:79]\n\t"
    "ds_write_b128 v13, v[122:125] offset:5120\n\t"
    "v_mfma_f32_32x32x16_bf16 v[64:79], v[98:101], a[212:215], v[64:79]\n\t"
    "ds_write_b128 v13, v[126:129] offset:6144\n\t"
    "v_mfma_f32_32x32x16_bf16 v[64:79], v[102:105], a[216:219], v[64:79]\n\t"
    "ds_write_b128 v13, v[130:133] offset:7168\n\t"
    "v_mfma_f32_32x32x16_bf16 v[64:79], v[106:109], a[220:223], v[64:79]\n\t"
    "s_waitcnt lgkmcnt(0)\n\t"
    "s_barrier\n\t"
    "ds_read_b128 v[118:121], v14 offset:4096\n\t"
    "ds_read_b128 v[134:137], v16 offset:32768\n\t"
    "ds_read_b128 v[150:153], v28 offset:36864\n\t"
    "ds_read_b128 v[122:125], v14 offset:5120\n\t"
    "ds_read_b128 v[138:141], v16 offset:33792\n\t"
    "ds_read_b128 v[154:157], v28 offset:37888\n\t"
    "ds_read_b128 v[126:129], v14 offset:6144\n\t"
    "ds_read_b128 v[142:145], v16 offset:34816\n\t"
    "ds_read_b128 v[158:161], v28 offset:38912\n\t"
    "ds_read_b128 v[130:133], v14 offset:7168\n\t"
    "ds_read_b128 v[146:149], v16 offset:35840\n\t"
    "ds_read_b128 v[162:165], v28 offset:39936\n\t"
    "s_waitcnt lgkmcnt(10)\n\t"
    "v_pk_add_f32 v[118:119], v[118:119], v[134:135]\n\t"
    "s_waitcnt lgkmcnt(9)\n\t"
    "v_pk_add_f32 v[64:65], v[64:65], v[150:151]\n\t"
    "v_pk_add_f32 v[120:121], v[120:121], v[136:137]\n\t"
    "v_pk_add_f32 v[66:67], v[66:67], v[152:153]\n\t"
    "v_pk_add_f32 v[64:65], v[64:65], v[118:119]\n\t"
    "v_pk_add_f32 v[66:67], v[66:67], v[120:121]\n\t"
    "v_cvt_pk_bf16_f32 v80, v64, v65\n\t"
    "v_cvt_pk_bf16_f32 v81, v66, v67\n\t"
    "v_dot2_f32_bf16 v7, v80, v36, 0\n\t"
    "v_dot2_f32_bf16 v8, v80, v80, 0\n\t"
    "v_dot2_f32_bf16 v7, v81, v36, v7\n\t"
    "v_dot2_f32_bf16 v8, v81, v81, v8\n\t"
    "s_waitcnt lgkmcnt(7)\n\t"
    "v_pk_add_f32 v[122:123], v[122:123], v[138:139]\n\t"
    "s_waitcnt lgkmcnt(6)\n\t"
    "v_pk_add_f32 v[68:69], v[68:69], v[154:155]\n\t"
    "v_pk_add_f32 v[124:125], v[124:125], v[140:141]\n\t"
    "v_pk_add_f32 v[70:71], v[70:71], v[156:157]\n\t"
    "v_pk_add_f32 v[68:69], v[68:69], v[122:123]\n\t"
    "v_pk_add_f32 v[70:71], v[70:71], v[124:125]\n\t"
    "v_cvt_pk_bf16_f32 v82, v68, v69\n\t"
    "v_cvt_pk_bf16_f32 v83, v70, v71\n\t"
    "buffer_store_dwordx4 v[80:83], v19, s[28:31], s39 offen offset:0 sc1\n\t"
    "v_dot2_f32_bf16 v7, v82, v36, v7\n\t"
    "v_dot2_f32_bf16 v8, v82, v82, v8\n\t"
    "v_dot2_f32_bf16 v7, v83, v36, v7\n\t"
    "v_dot2_f32_bf16 v8, v83, v83, v8\n\t"
    "s_waitcnt lgkmcnt(4)\n\t"
    "v_pk_add_f32 v[126:127], v[126:127], v[142:143]\n\t"
    "s_waitcnt lgkmcnt(3)\n\t"
    "v_pk_add_f32 v[72:73], v[72:73], v[158:159]\n\t"
    "v_pk_add_f32 v[128:129], v[128:129], v[144:145]\n\t"
    "v_pk_add_f32 v[74:75], v[74:75], v[160:161]\n\t"
    "v_pk_add_f32 v[72:73], v[72:73], v[126:127]\n\t"
    "v_pk_add_f32 v[74:75], v[74:75], v[128:129]\n\t"
    "v_cvt_pk_bf16_f32 v84, v72, v73\n\t"
    "v_cvt_pk_bf16_f32 v85, v74, v75\n\t"
    "v_dot2_f32_bf16 v7, v84, v36, v7\n\t"
    "v_dot2_f32_bf16 v8, v84, v84, v8\n\t"
    "v_dot2_f32_bf16 v7, v85, v36, v7\n\t"
    "v_dot2_f32_bf16 v8, v85, v85, v8\n\t"
    "s_waitcnt lgkmcnt(1)\n\t"
    "v_pk_add_f32 v[130:131], v[130:131], v[146:147]\n\t"
    "s_waitcnt lgkmcnt(0)\n\t"
    "v_pk_add_f32 v[76:77], v[76:77], v[162:163]\n\t"
    "v_pk_add_f32 v[132:133], v[132:133], v[148:149]\n\t"
    "v_pk_add_f32 v[78:79], v[78:79], v[164:165]\n\t"
    "v_pk_add_f32 v[76:77], v[76:77], v[130:131]\n\t"
    "v_pk_add_f32 v[78:79], v[78:79], v[132:133]\n\t"
    "v_cvt_pk_bf16_f32 v86, v76, v77\n\t"
    "v_cvt_pk_bf16_f32 v87, v78, v79\n\t"
    "buffer_store_dwordx4 v[84:87], v19, s[28:31], s39 offen offset:1024 sc1\n\t"
    "v_dot2_f32_bf16 v7, v86, v36, v7\n\t"
    "v_dot2_f32_bf16 v8, v86, v86, v8\n\t"
    "v_dot2_f32_bf16 v7, v87, v36, v7\n\t"
    "v_dot2_f32_bf16 v8, v87, v87, v8\n\t"
    "s_nop 2\n\t"
    "v_permlane32_swap_b32 v7, v8\n\t"
    "v_add_f32 v7, v7, v8\n\t"
    "buffer_store_dword v7, v9, s[28:31], s44 offen sc1\n\t"
    "s_lshl3_add_u32 s48, s64, s65\n\t"
    "s_mul_i32 s48, s48, 128\n\t"
    "s_add_u32 s48, s48, 131072\n\t"
    "s_add_u32 m0, s63, 135168\n\t"
    "s_add_u32 s42, s84, 114688\n\t"
    "buffer_load_dwordx4 v42, s[20:23], s42 offen lds\n\t"
    "s_add_u32 m0, s63, 136192\n\t"
    "s_nop 0\n\t"
    "buffer_load_dwordx4 v42, s[24:27], s42 offen lds\n\t"
    "s_waitcnt vmcnt(2)\n\t"
    "s_barrier\n\t"
    "s_cmp_lg_u32 s33, 1\n\t"
    "s_cbranch_scc1 L_pub4_%=\n\t"
    "s_mov_b64 exec, 1\n\t"
    "buffer_store_dword v2, off, s[28:31], s48 sc1\n\t"
    "s_mov_b64 exec, -1\n\t"
    "L_pub4_%=:\n\t"
    "s_cmp_lt_u32 s36, 28\n\t"
    "s_cbranch_scc1 L_pf5_%=\n\t"
    "buffer_load_dword v15, v45, s[8:11], 0 offen offset:0\n\t"
    "buffer_load_dword v15, v45, s[8:11], 0 offen offset:128\n\t"
    "s_mov_b32 s22, 0\n\t"
    "s_mov_b32 s26, 0\n\t"
    "L_pf5_%=:\n\t"
    "s_mul_i32 s48, s77, 2048\n\t"
    "s_add_u32 s48, s48, 131072\n\t"
    "v_mul_u32_u24 v3, 128, v11\n\t"
    "s_add_u32 m0, s63, 32768\n\t"
    "s_add_u32 s42, s84, 172032\n\t"
    "buffer_load_dwordx4 v42, s[20:23], s42 offen lds\n\t"
    "s_mov_b64 s[16:17], s[28:29]\n\t"
    "s_cmp_lt_u32 s36, 28\n\t"
    "s_cselect_b32 s18, 12582912, 0\n\t"
    "s_lshl_b32 s40, s77, 17\n\t"
    "s_add_u32 m0, s63, 33792\n\t"
    "s_nop 0\n\t"
    "buffer_load_dwordx4 v42, s[24:27], s42 offen lds\n\t"
    "s_add_u32 s40, s40, 3145728\n\t"
    "s_lshl1_add_u32 s39, s33, s65\n\t"
    "s_and_b32 s39, s39, 7\n\t"
    "s_lshl_b32 s41, s39, 12\n\t"
    "s_add_u32 s44, s40, s41\n\t"
    "s_add_u32 m0, s63, 34816\n\t"
    "s_add_u32 s42, s85, 0\n\t"
    "buffer_load_dwordx4 v42, s[20:23], s42 offen lds\n\t"
    "s_add_u32 s39, s39, 1\n\t"
    "s_and_b32 s39, s39, 7\n\t"
    "s_lshl_b32 s41, s39, 12\n\t"
    "s_add_u32 s46, s40, s41\n\t"
    "v_add_u32 v5, 65536, v19\n\t"
    "v_add_u32 v45, 7168, v41\n\t"
    "s_add_u32 m0, s63, 35840\n\t"
    "s_nop 0\n\t"
    "buffer_load_dwordx4 v42, s[24:27], s42 offen lds\n\t"
    "v_bfe_u32 v7, v1, 2, 2\n\t"
    "v_lshl_add_u32 v8, v18, 2, v7\n\t"
    "v_lshlrev_b32 v8, 7, v8\n\t"
    "v_xor_b32 v9, s33, v12\n\t"
    "v_and_b32 v9, 1, v9\n\t"
    "s_add_u32 m0, s63, 36864\n\t"
    "s_add_u32 s42, s85, 57344\n\t"
    "buffer_load_dwordx4 v42, s[20:23], s42 offen lds\n\t"
    "v_lshl_add_u32 v8, v9, 5, v8\n\t"
    "v_and_b32 v9, 3, v1\n\t"
    "v_lshl_add_u32 v8, v9, 3, v8\n\t"
    "v_xor_b32 v7, s33, v7\n\t"
    "v_bfe_u32 v7, v7, 1, 1\n\t"
    "v_lshl_add_u32 v8, v7, 6, v8\n\t"
    "s_add_u32 m0, s63, 37888\n\t"
    "s_nop 0\n\t"
    "buffer_load_dwordx4 v42, s[24:27], s42 offen lds\n\t"
    "v_add_u32 v6, s63, v8\n\t"
    "v_xor_b32 v13, 64, v6\n\t"
    "v_add_u32 v14, 65536, v6\n\t"
    "v_add_u32 v16, 65536, v13\n\t"
    "v_add_u32 v40, 131072, v6\n\t"
    "s_add_u32 m0, s63, 38912\n\t"
    "s_add_u32 s42, s85, 114688\n\t"
    "buffer_load_dwordx4 v42, s[20:23], s42 offen lds\n\t"
    "v_add_u32 v43, 131072, v13\n\t"
    "v_lshlrev_b32 v44, 4, v17\n\t"
    "v_add_u32 v44, 137216, v44\n\t"
    "s_mul_i32 s45, s77, 458752\n\t"
    "s_lshl2_add_u32 s39, s76, s33\n\t"
    "s_lshl_b32 s39, s39, 11\n\t"
    "s_add_u32 m0, s63, 39936\n\t"
    "s_nop 0\n\t"
    "buffer_load_dwordx4 v42, s[24:27], s42 offen lds\n\t"
    "s_add_u32 s45, s45, s39\n\t"
    "s_add_u32 s45, s45, 5242880\n\t"
    "s_cmp_lt_u32 s36, 28\n\t"
    "s_cselect_b32 s39, s76, 15\n\t"
    "s_lshl4_add_u32 s47, s77, s39\n\t"
    "s_mul_i32 s47, s47, 128\n\t"
    "s_add_u32 m0, s63, 137216\n\t"
    "s_add_u32 s42, s85, 172032\n\t"
    "buffer_load_dwordx4 v42, s[20:23], s42 offen lds\n\t"
    "s_add_u32 s47, s47, 196608\n\t"
    "s_lshr_b32 s39, s36, 4\n\t"
    "s_lshl1_add_u32 s39, s35, s39\n\t"
    "s_lshl2_add_u32 s39, s34, s39\n\t"
    "s_bfe_u32 s40, s36, 0x10003\n\t"
    "s_lshl2_add_u32 s49, s39, s40\n\t"
    "s_add_u32 m0, s63, 138240\n\t"
    "s_nop 0\n\t"
    "buffer_load_dwordx4 v42, s[24:27], s42 offen lds\n\t"
    "s_mul_i32 s40, s33, 7\n\t"
    "s_lshr_b32 s40, s40, 1\n\t"
    "s_lshl4_add_u32 s58, s39, s40\n\t"
    "s_mul_i32 s58, s58, 128\n\t"
    "s_add_u32 s58, s58, 196608\n\t"
    "s_add_u32 m0, s63, 65536\n\t"
    "s_add_u32 s42, s86, 0\n\t"
    "buffer_load_dwordx4 v42, s[20:23], s42 offen lds\n\t"
    "s_mov_b32 s41, 0\n\t"
    "s_add_u32 s39, s41, 0\n\t"
    "s_and_b32 s39, s39, 3\n\t"
    "s_lshl2_add_u32 s39, s33, s39\n\t"
    "s_mul_i32 s39, s39, 7168\n\t"
    "s_mul_i32 s40, s49, 114688\n\t"
    "s_add_u32 m0, s63, 66560\n\t"
    "s_nop 0\n\t"
    "buffer_load_dwordx4 v42, s[24:27], s42 offen lds\n\t"
    "s_add_u32 s54, s39, s40\n\t"
    "s_add_u32 s54, s54, 5242880\n\t"
    "s_lshl_b32 s39, s39, 4\n\t"
    "s_lshl_b32 s40, s65, 7\n\t"
    "s_add_u32 s50, s39, s40\n\t"
    "s_add_u32 m0, s63, 67584\n\t"
    "s_add_u32 s42, s86, 57344\n\t"
    "buffer_load_dwordx4 v42, s[20:23], s42 offen lds\n\t"
    "s_add_u32 s39, s41, 1\n\t"
    "s_and_b32 s39, s39, 3\n\t"
    "s_lshl2_add_u32 s39, s33, s39\n\t"
    "s_mul_i32 s39, s39, 7168\n\t"
    "s_mul_i32 s40, s49, 114688\n\t"
    "s_add_u32 s55, s39, s40\n\t"
    "s_add_u32 m0, s63, 68608\n\t"
    "s_nop 0\n\t"
    "buffer_load_dwordx4 v42, s[24:27], s42 offen lds\n\t"
    "s_add_u32 s55, s55, 5242880\n\t"
    "s_lshl_b32 s39, s39, 4\n\t"
    "s_lshl_b32 s40, s65, 7\n\t"
    "s_add_u32 s51, s39, s40\n\t"
    "s_add_u32 s39, s41, 2\n\t"
    "s_add_u32 m0, s63, 69632\n\t"
    "s_add_u32 s42, s86, 114688\n\t"
    "buffer_load_dwordx4 v42, s[20:23], s42 offen lds\n\t"
    "s_and_b32 s39, s39, 3\n\t"
    "s_lshl2_add_u32 s39, s33, s39\n\t"
    "s_mul_i32 s39, s39, 7168\n\t"
    "s_mul_i32 s40, s49, 114688\n\t"
    "s_add_u32 s56, s39, s40\n\t"
    "s_add_u32 s56, s56, 5242880\n\t"
    "s_add_u32 m0, s63, 70656\n\t"
    "s_nop 0\n\t"
    "buffer_load_dwordx4 v42, s[24:27], s42 offen lds\n\t"
    "s_lshl_b32 s39, s39, 4\n\t"
    "s_lshl_b32 s40, s65, 7\n\t"
    "s_add_u32 s52, s39, s40\n\t"
    "s_add_u32 s39, s41, 3\n\t"
    "s_and_b32 s39, s39, 3\n\t"
    "s_add_u32 m0, s63, 71680\n\t"
    "s_add_u32 s42, s86, 172032\n\t"
    "buffer_load_dwordx4 v42, s[20:23], s42 offen lds\n\t"
    "s_lshl2_add_u32 s39, s33, s39\n\t"
    "s_mul_i32 s39, s39, 7168\n\t"
    "s_mul_i32 s40, s49, 114688\n\t"
    "s_add_u32 s57, s39, s40\n\t"
    "s_add_u32 s57, s57, 5242880\n\t"
    "s_lshl_b32 s39, s39, 4\n\t"
    "s_add_u32 m0, s63, 72704\n\t"
    "s_nop 0\n\t"
    "buffer_load_dwordx4 v42, s[24:27], s42 offen lds\n\t"
    "s_lshl_b32 s40, s65, 7\n\t"
    "s_add_u32 s53, s39, s40\n\t"
    "s_lshl2_add_u32 s39, s77, s33\n\t"
    "s_lshl_b32 s39, s39, 12\n\t"
    "s_add_u32 s39, s39, 524288\n\t"
    "s_waitcnt lgkmcnt(0)\n\t"
    "ds_read_b64_tr_b16 a[160:161], v48 offset:0\n\t"
    "ds_read_b64_tr_b16 a[162:163], v48 offset:512\n\t"
    "ds_read_b64_tr_b16 a[164:165], v48 offset:1024\n\t"
    "ds_read_b64_tr_b16 a[166:167], v48 offset:1536\n\t"
    "ds_read_b64_tr_b16 a[168:169], v48 offset:2048\n\t"
    "ds_read_b64_tr_b16 a[170:171], v48 offset:2560\n\t"
    "ds_read_b64_tr_b16 a[172:173], v48 offset:3072\n\t"
    "ds_read_b64_tr_b16 a[174:175], v48 offset:3584\n\t"
    "s_waitcnt lgkmcnt(6)\n\t"
    "v_mfma_f32_16x16x32_bf16 v[52:55], v[60:63], a[160:163], v[52:55]\n\t"
    "s_waitcnt vmcnt(18)\n\t"
    "ds_read_b64_tr_b16 a[176:177], v48 offset:4096\n\t"
    "ds_read_b64_tr_b16 a[178:179], v48 offset:4608\n\t"
    "s_waitcnt lgkmcnt(6)\n\t"
    "v_mfma_f32_16x16x32_bf16 v[56:59], v[60:63], a[164:167], v[56:59]\n\t"
    "ds_read_b64_tr_b16 a[180:181], v48 offset:5120\n\t"
    "ds_read_b64_tr_b16 a[182:183], v48 offset:5632\n\t"
    "s_waitcnt lgkmcnt(6)\n\t"
    "v_mfma_f32_16x16x32_bf16 v[52:55], v[60:63], a[168:171], v[52:55]\n\t"
    "s_waitcnt lgkmcnt(4)\n\t"
    "v_mfma_f32_16x16x32_bf16 v[56:59], v[60:63], a[172:175], v[56:59]\n\t"
    "s_waitcnt lgkmcnt(2)\n\t"
    "v_mfma_f32_16x16x32_bf16 v[52:55], v[60:63], a[176:179], v[52:55]\n\t"
    "s_waitcnt lgkmcnt(0)\n\t"
    "v_mfma_f32_16x16x32_bf16 v[56:59], v[60:63], a[180:183], v[56:59]\n\t"
    "buffer_load_dword v4, v3, s[28:31], s48 offen sc1\n\t"
    "s_waitcnt vmcnt(17)\n\t"
    "ds_read_b64_tr_b16 a[184:185], v46 offset:32768\n\t"
    "ds_read_b64_tr_b16 a[186:187], v46 offset:33280\n\t"
    "ds_read_b64_tr_b16 a[188:189], v46 offset:33792\n\t"
    "ds_read_b64_tr_b16 a[190:191], v46 offset:34304\n\t"
    "s_waitcnt vmcnt(15)\n\t"
    "ds_read_b64_tr_b16 a[192:193], v46 offset:34816\n\t"
    "ds_read_b64_tr_b16 a[194:195], v46 offset:35328\n\t"
    "ds_read_b64_tr_b16 a[196:197], v46 offset:35840\n\t"
    "ds_read_b64_tr_b16 a[198:199], v46 offset:36352\n\t"
    "s_waitcnt lgkmcnt(6)\n\t"
    "v_mfma_f32_16x16x32_bf16 v[52:55], v[60:63], a[184:187], v[52:55]\n\t"
    "s_waitcnt vmcnt(13)\n\t"
    "ds_read_b64_tr_b16 a[200:201], v46 offset:36864\n\t"
    "ds_read_b64_tr_b16 a[202:203], v46 offset:37376\n\t"
    "s_waitcnt lgkmcnt(6)\n\t"
    "v_mfma_f32_16x16x32_bf16 v[56:59], v[60:63], a[188:191], v[56:59]\n\t"
    "ds_read_b64_tr_b16 a[204:205], v46 offset:37888\n\t"
    "ds_read_b64_tr_b16 a[206:207], v46 offset:38400\n\t"
    "s_waitcnt lgkmcnt(6)\n\t"
    "v_mfma_f32_16x16x32_bf16 v[52:55], v[60:63], a[192:195], v[52:55]\n\t"
    "s_waitcnt vmcnt(11)\n\t"
    "ds_read_b64_tr_b16 a[208:209], v46 offset:38912\n\t"
    "ds_read_b64_tr_b16 a[210:211], v46 offset:39424\n\t"
    "s_waitcnt lgkmcnt(6)\n\t"
    "v_mfma_f32_16x16x32_bf16 v[56:59], v[60:63], a[196:199], v[56:59]\n\t"
    "ds_read_b64_tr_b16 a[212:213], v46 offset:39936\n\t"
    "ds_read_b64_tr_b16 a[214:215], v46 offset:40448\n\t"
    "s_waitcnt lgkmcnt(6)\n\t"
    "v_mfma_f32_16x16x32_bf16 v[52:55], v[60:63], a[200:203], v[52:55]\n\t"
    "s_waitcnt lgkmcnt(4)\n\t"
    "v_mfma_f32_16x16x32_bf16 v[56:59], v[60:63], a[204:207], v[56:59]\n\t"
    "s_waitcnt lgkmcnt(2)\n\t"
    "v_mfma_f32_16x16x32_bf16 v[52:55], v[60:63], a[208:211], v[52:55]\n\t"
    "s_waitcnt lgkmcnt(0)\n\t"
    "v_mfma_f32_16x16x32_bf16 v[56:59], v[60:63], a[212:215], v[56:59]\n\t"
    "s_waitcnt lgkmcnt(0)\n\t"
    "s_waitcnt vmcnt(0)\n\t"
    "v_cmp_ne_u32 vcc, v4, v2\n\t"
    "s_mov_b32 s41, 40000\n\t"
    "s_nop 3\n\t"
    "s_cbranch_vccz L_qe6_%=\n\t"
    "L_ql7_%=:\n\t"
    "buffer_load_dword v4, v3, s[28:31], s48 offen sc1\n\t"
    "s_sub_u32 s41, s41, 1\n\t"
    "s_cbranch_scc1 L_qe6_%=\n\t"
    "s_waitcnt vmcnt(0)\n\t"
    "v_cmp_ne_u32 vcc, v4, v2\n\t"
    "s_nop 4\n\t"
    "s_cbranch_vccnz L_ql7_%=\n\t"
    "L_qe6_%=:\n\t"
    "s_add_u32 m0, s63, 0\n\t"
    "s_add_u32 s42, s44, 0\n\t"
    "buffer_load_dwordx4 v19, s[16:19], s42 offen offset:0 sc1 lds\n\t"
    "buffer_load_dwordx4 v19, s[16:19], s42 offen offset:1024 sc1 lds\n\t"
    "buffer_load_dwordx4 v19, s[16:19], s42 offen offset:2048 sc1 lds\n\t"
    "buffer_load_dwordx4 v19, s[16:19], s42 offen offset:3072 sc1 lds\n\t"
    "s_add_u32 m0, s63, 4096\n\t"
    "s_add_u32 s42, s46, 0\n\t"
    "buffer_load_dwordx4 v19, s[16:19], s42 offen offset:0 sc1 lds\n\t"
    "buffer_load_dwordx4 v19, s[16:19], s42 offen offset:1024 sc1 lds\n\t"
    "buffer_load_dwordx4 v19, s[16:19], s42 offen offset:2048 sc1 lds\n\t"
    "buffer_load_dwordx4 v19, s[16:19], s42 offen offset:3072 sc1 lds\n\t"
    "buffer_load_dwordx4 v[80:83], v19, s[16:19], s39 offen offset:0 sc1\n\t"
    "buffer_load_dwordx4 v[84:87], v19, s[16:19], s39 offen offset:1024 sc1\n\t"
    "buffer_load_dwordx4 v[88:91], v19, s[16:19], s39 offen offset:2048 sc1\n\t"
    "buffer_load_dwordx4 v[92:95], v19, s[16:19], s39 offen offset:3072 sc1\n\t"
    "ds_read_b64_tr_b16 a[216:217], v48 offset:6144\n\t"
    "ds_read_b64_tr_b16 a[218:219], v48 offset:6656\n\t"
    "ds_read_b64_tr_b16 a[220:221], v48 offset:7168\n\t"
    "ds_read_b64_tr_b16 a[222:223], v48 offset:7680\n\t"
    "ds_read_b64_tr_b16 a[224:225], v47 offset:0\n\t"
    "ds_read_b64_tr_b16 a[226:227], v47 offset:512\n\t"
    "ds_read_b64_tr_b16 a[228:229], v47 offset:1024\n\t"
    "ds_read_b64_tr_b16 a[230:231], v47 offset:1536\n\t"
    "s_waitcnt lgkmcnt(6)\n\t"
    "v_mfma_f32_16x16x32_bf16 v[52:55], v[60:63], a[216:219], v[52:55]\n\t"
    "ds_read_b64_tr_b16 a[232:233], v47 offset:2048\n\t"
    "ds_read_b64_tr_b16 a[234:235], v47 offset:2560\n\t"
    "s_waitcnt lgkmcnt(6)\n\t"
    "v_mfma_f32_16x16x32_bf16 v[56:59], v[60:63], a[220:223], v[56:59]\n\t"
    "ds_read_b64_tr_b16 a[236:237], v47 offset:3072\n\t"
    "ds_read_b64_tr_b16 a[238:239], v47 offset:3584\n\t"
    "s_waitcnt lgkmcnt(6)\n\t"
    "v_mfma_f32_16x16x32_bf16 v[52:55], v[60:63], a[224:227], v[52:55]\n\t"
    "ds_read_b64_tr_b16 a[240:241], v47 offset:4096\n\t"
    "ds_read_b64_tr_b16 a[242:243], v47 offset:4608\n\t"
    "s_waitcnt lgkmcnt(6)\n\t"
    "v_mfma_f32_16x16x32_bf16 v[56:59], v[60:63], a[228:231], v[56:59]\n\t"
    "ds_read_b64_tr_b16 a[244:245], v47 offset:5120\n\t"
    "ds_read_b64_tr_b16 a[246:247], v47 offset:5632\n\t"
    "s_waitcnt lgkmcnt(6)\n\t"
    "v_mfma_f32_16x16x32_bf16 v[52:55], v[60:63], a[232:235], v[52:55]\n\t"
    "ds_read_b64_tr_b16 a[248:249], v47 offset:6144\n\t"
    "ds_read_b64_tr_b16 a[250:251], v47 offset:6656\n\t"
    "s_waitcnt lgkmcnt(6)\n\t"
    "v_mfma_f32_16x16x32_bf16 v[56:59], v[60:63], a[236:239], v[56:59]\n\t"
    "ds_read_b64_tr_b16 a[252:253], v47 offset:7168\n\t"
    "ds_read_b64_tr_b16 a[254:255], v47 offset:7680\n\t"
    "s_waitcnt lgkmcnt(6)\n\t"
    "v_mfma_f32_16x16x32_bf16 v[52:55], v[60:63], a[240:243], v[52:55]\n\t"
    "s_waitcnt lgkmcnt(4)\n\t"
    "v_mfma_f32_16x16x32_bf16 v[56:59], v[60:63], a[244:247], v[56:59]\n\t"
    "s_waitcnt lgkmcnt(2)\n\t"
    "v_mfma_f32_16x16x32_bf16 v[52:55], v[60:63], a[248:251], v[52:55]\n\t"
    "s_waitcnt lgkmcnt(0)\n\t"
    "v_mfma_f32_16x16x32_bf16 v[56:59], v[60:63], a[252:255], v[56:59]\n\t"
    "s_waitcnt lgkmcnt(0)\n\t"
    "v_cmp_eq_u32 vcc, 0, v18\n\t"
    "s_nop 3\n\t"
    "v_cvt_pk_bf16_f32 v7, v52, v52\n\t"
    "v_and_b32 v7, 0xffff0000, v7\n\t"
    "v_sub_f32 v7, v52, v7\n\t"
    "v_cvt_pk_bf16_f32 v7, v52, v7\n\t"
    "v_cndmask_b32 v36, 0, v7, vcc\n\t"
    "v_cndmask_b32 v37, 0, v7, vcc\n\t"
    "v_mov_b32 v38, 0\n\t"
    "v_mov_b32 v39, 0\n\t"
    "v_cvt_pk_bf16_f32 v7, v56, v56\n\t"
    "v_and_b32 v7, 0xffff0000, v7\n\t"
    "v_sub_f32 v7, v56, v7\n\t"
    "v_cvt_pk_bf16_f32 v7, v56, v7\n\t"
    "v_cndmask_b32 v46, 0, v7, vcc\n\t"
    "v_cndmask_b32 v47, 0, v7, vcc\n\t"
    "v_mov_b32 v48, 0\n\t"
    "v_mov_b32 v49, 0\n\t"
    "v_min_u32 v3, 3, v1\n\t"
    "v_mul_u32_u24 v3, 128, v3\n\t"
    "v_add_u32 v42, 4096, v19\n\t"
    "s_add_u32 s59, s54, 229376\n\t"
    "s_add_u32 s60, s55, 229376\n\t"
    "s_add_u32 s61, s56, 229376\n\t"
    "s_add_u32 s62, s57, 229376\n\t"
    "s_lshl2_add_u32 s39, s43, s33\n\t"
    "s_cmp_lg_u32 s39, 1020\n\t"
    "s_cbranch_scc1 L_ep10_%=\n\t"
    "v_mul_u32_u24 v7, 512, v1\n\t"
    "s_mov_b32 s41, 40000\n\t"
    "s_mov_b32 s40, 65536\n\t"
    "L_epl11_%=:\n\t"
    "buffer_load_dword v112, v7, s[28:31], s40 offen offset:0 sc1\n\t"
    "buffer_load_dword v113, v7, s[28:31], s40 offen offset:128 sc1\n\t"
    "buffer_load_dword v114, v7, s[28:31], s40 offen offset:256 sc1\n\t"
    "buffer_load_dword v115, v7, s[28:31], s40 offen offset:384 sc1\n\t"
    "s_sub_u32 s41, s41, 1\n\t"
    "s_cbranch_scc1 L_ep10_%=\n\t"
    "s_waitcnt vmcnt(0)\n\t"
    "v_xor_b32 v112, v112, v2\n\t"
    "v_xor_b32 v113, v113, v2\n\t"
    "v_xor_b32 v114, v114, v2\n\t"
    "v_xor_b32 v115, v115, v2\n\t"
    "v_or3_b32 v112, v112, v113, v114\n\t"
    "v_or_b32 v112, v112, v115\n\t"
    "v_cmp_ne_u32 vcc, 0, v112\n\t"
    "s_nop 4\n\t"
    "s_cbranch_vccnz L_epl11_%=\n\t"
    "s_add_u32 s39, s38, 1\n\t"
    "v_mov_b32 v112, s39\n\t"
    "s_mov_b64 exec, 1\n\t"
    "buffer_store_dword v112, off, s[28:31], 0 sc1\n\t"
    "s_mov_b64 exec, -1\n\t"
    "L_ep10_%=:\n\t"
    "s_waitcnt vmcnt(8)\n\t"
    "s_barrier\n\t"
    "ds_read_b128 v[112:115], v19 offset:0\n\t"
    "ds_read_b128 v[116:119], v19 offset:1024\n\t"
    "ds_read_b128 v[120:123], v19 offset:2048\n\t"
    "ds_read_b128 v[124:127], v19 offset:3072\n\t"
    "ds_read_b128 v[128:131], v19 offset:8192\n\t"
    "ds_read_b128 v[132:135], v19 offset:9216\n\t"
    "ds_read_b128 v[136:139], v19 offset:10240\n\t"
    "s_waitcnt lgkmcnt(6)\n\t"
    "v_mfma_f32_32x32x16_bf16 v[20:35], a[0:3], v[112:115], 0\n\t"
    "ds_read_b128 v[140:143], v19 offset:11264\n\t"
    "s_add_u32 m0, s63, 32768\n\t"
    "s_add_u32 s42, s44, 32768\n\t"
    "buffer_load_dwordx4 v19, s[16:19], s42 offen offset:0 sc1 lds\n\t"
    "v_mfma_f32_32x32x16_bf16 v[64:79], a[4:7], v[112:115], 0\n\t"
    "s_waitcnt lgkmcnt(6)\n\t"
    "v_mfma_f32_32x32x16_bf16 v[20:35], a[8:11], v[116:119], v[20:35]\n\t"
    "ds_read_b128 v[112:115], v19 offset:16384\n\t"
    "buffer_load_dwordx4 v19, s[16:19], s42 offen offset:1024 sc1 lds\n\t"
    "v_mfma_f32_32x32x16_bf16 v[64:79], a[12:15], v[116:119], v[64:79]\n\t"
    "s_waitcnt lgkmcnt(5)\n\t"
    "v_mfma_f32_32x32x16_bf16 v[20:35], a[16:19], v[120:123], v[20:35]\n\t"
    "ds_read_b128 v[116:119], v19 offset:17408\n\t"
    "v_mfma_f32_32x32x16_bf16 v[64:79], a[20:23], v[120:123], v[64:79]\n\t"
    "buffer_load_dwordx4 v19, s[16:19], s42 offen offset:2048 sc1 lds\n\t"
    "v_mfma_f32_32x32x16_bf16 v[20:35], a[24:27], v[124:127], v[20:35]\n\t"
    "ds_read_b128 v[120:123], v19 offset:18432\n\t"
    "v_mfma_f32_32x32x16_bf16 v[64:79], a[28:31], v[124:127], v[64:79]\n\t"
    "buffer_load_dwordx4 v19, s[16:19], s42 offen offset:3072 sc1 lds\n\t"
    "s_waitcnt lgkmcnt(5)\n\t"
    "v_mfma_f32_32x32x16_bf16 v[20:35], a[64:67], v[128:131], v[20:35]\n\t"
    "ds_read_b128 v[124:127], v19 offset:19456\n\t"
    "v_mfma_f32_32x32x16_bf16 v[64:79], a[68:71], v[128:131], v[64:79]\n\t"
    "v_mfma_f32_32x32x16_bf16 v[20:35], a[72:75], v[132:135], v[20:35]\n\t"
    "ds_read_b128 v[128:131], v19 offset:24576\n\t"
    "s_add_u32 m0, s63, 36864\n\t"
    "s_add_u32 s42, s46, 32768\n\t"
    "buffer_load_dwordx4 v19, s[16:19], s42 offen offset:0 sc1 lds\n\t"
    "v_mfma_f32_32x32x16_bf16 v[64:79], a[76:79], v[132:135], v[64:79]\n\t"
    "s_waitcnt lgkmcnt(5)\n\t"
    "v_mfma_f32_32x32x16_bf16 v[20:35], a[80:83], v[136:139], v[20:35]\n\t"
    "ds_read_b128 v[132:135], v19 offset:25600\n\t"
    "buffer_load_dwordx4 v19, s[16:19], s42 offen offset:1024 sc1 lds\n\t"
    "v_mfma_f32_32x32x16_bf16 v[64:79], a[84:87], v[136:139], v[64:79]\n\t"
    "v_mfma_f32_32x32x16_bf16 v[20:35], a[88:91], v[140:143], v[20:35]\n\t"
    "ds_read_b128 v[136:139], v19 offset:26624\n\t"
    "v_mfma_f32_32x32x16_bf16 v[64:79], a[92:95], v[140:143], v[64:79]\n\t"
    "buffer_load_dwordx4 v19, s[16:19], s42 offen offset:2048 sc1 lds\n\t"
    "s_waitcnt lgkmcnt(5)\n\t"
    "v_mfma_f32_32x32x16_bf16 v[20:35], a[128:131], v[112:115], v[20:35]\n\t"
    "ds_read_b128 v[140:143], v19 offset:27648\n\t"
    "s_waitcnt vmcnt(9)\n\t"
    "v_add_f32 v80, v80, v84\n\t"
    "v_add_f32 v81, v81, v85\n\t"
    "v_mfma_f32_32x32x16_bf16 v[64:79], a[132:135], v[112:115], v[64:79]\n\t"
    "buffer_load_dwordx4 v19, s[16:19], s42 offen offset:3072 sc1 lds\n\t"
    "v_mfma_f32_32x32x16_bf16 v[20:35], a[136:139], v[116:119], v[20:35]\n\t"
    "s_barrier\n\t"
    "ds_read_b128 v[112:115], v19 offset:4096\n\t"
    "v_mfma_f32_32x32x16_bf16 v[64:79], a[140:143], v[116:119], v[64:79]\n\t"
    "v_add_f32 v82, v82, v86\n\t"
    "v_add_f32 v83, v83, v87\n\t"
    "s_waitcnt lgkmcnt(5)\n\t"
    "v_mfma_f32_32x32x16_bf16 v[20:35], a[144:147], v[120:123], v[20:35]\n\t"
    "ds_read_b128 v[116:119], v19 offset:5120\n\t"
    "s_add_u32 m0, s63, 65536\n\t"
    "s_add_u32 s42, s44, 65536\n\t"
    "buffer_load_dwordx4 v19, s[16:19], s42 offen offset:0 sc1 lds\n\t"
    "v_mfma_f32_32x32x16_bf16 v[64:79], a[148:151], v[120:123], v[64:79]\n\t"
    "s_waitcnt vmcnt(9)\n\t"
    "v_add_f32 v88, v88, v92\n\t"
    "v_add_f32 v89, v89, v93\n\t"
    "v_mfma_f32_32x32x16_bf16 v[20:35], a[152:155], v[124:127], v[20:35]\n\t"
    "ds_read_b128 v[120:123], v19 offset:6144\n\t"
    "buffer_load_dwordx4 v19, s[16:19], s42 offen offset:1024 sc1 lds\n\t"
    "v_mfma_f32_32x32x16_bf16 v[64:79], a[156:159], v[124:127], v[64:79]\n\t"
    "v_add_f32 v90, v90, v94\n\t"
    "v_add_f32 v91, v91, v95\n\t"
    "s_waitcnt lgkmcnt(5)\n\t"
    "v_mfma_f32_32x32x16_bf16 v[20:35], a[192:195], v[128:131], v[20:35]\n\t"
    "ds_read_b128 v[124:127], v19 offset:7168\n\t"
    "v_add_f32 v80, v80, v88\n\t"
    "v_add_f32 v81, v81, v89\n\t"
    "v_mfma_f32_32x32x16_bf16 v[64:79], a[196:199], v[128:131], v[64:79]\n\t"
    "buffer_load_dwordx4 v19, s[16:19], s42 offen offset:2048 sc1 lds\n\t"
    "v_mfma_f32_32x32x16_bf16 v[20:35], a[200:203], v[132:135], v[20:35]\n\t"
    "ds_read_b128 v[128:131], v19 offset:12288\n\t"
    "v_add_f32 v82, v82, v90\n\t"
    "v_add_f32 v83, v83, v91\n\t"
    "v_mfma_f32_32x32x16_bf16 v[64:79], a[204:207], v[132:135], v[64:79]\n\t"
    "buffer_load_dwordx4 v19, s[16:19], s42 offen offset:3072 sc1 lds\n\t"
    "s_waitcnt lgkmcnt(5)\n\t"
    "v_mfma_f32_32x32x16_bf16 v[20:35], a[208:211], v[136:139], v[20:35]\n\t"
    "ds_read_b128 v[132:135], v19 offset:13312\n\t"
    "v_permlane32_swap_b32 v80, v81\n\t"
    "v_mfma_f32_32x32x16_bf16 v[64:79], a[212:215], v[136:139], v[64:79]\n\t"
    "v_permlane32_swap_b32 v82, v83\n\t"
    "v_add_f32 v80, v80, v81\n\t"
    "v_add_f32 v82, v82, v83\n\t"
    "v_mfma_f32_32x32x16_bf16 v[20:35], a[216:219], v[140:143], v[20:35]\n\t"
    "ds_read_b128 v[136:139], v19 offset:14336\n\t"
    "s_add_u32 m0, s63, 69632\n\t"
    "s_add_u32 s42, s46, 65536\n\t"
    "buffer_load_dwordx4 v19, s[16:19], s42 offen offset:0 sc1 lds\n\t"
    "v_mfma_f32_32x32x16_bf16 v[64:79], a[220:223], v[140:143], v[64:79]\n\t"
    "v_lshlrev_b32 v88, 6, v11\n\t"
    "v_permlane16_swap_b32 v80, v82\n\t"
    "s_waitcnt lgkmcnt(5)\n\t"
    "v_mfma_f32_32x32x16_bf16 v[20:35], a[32:35], v[112:115], v[20:35]\n\t"
    "ds_read_b128 v[140:143], v19 offset:15360\n\t"
    "buffer_load_dwordx4 v19, s[16:19], s42 offen offset:1024 sc1 lds\n\t"
    "v_mfma_f32_32x32x16_bf16 v[64:79], a[36:39], v[112:115], v[64:79]\n\t"
    "v_and_b32 v89, 1, v12\n\t"
    "v_add_f32 v80, v80, v82\n\t"
    "v_lshl_add_u32 v88, v89, 5, v88\n\t"
    "v_mfma_f32_32x32x16_bf16 v[20:35], a[40:43], v[116:119], v[20:35]\n\t"
    "ds_read_b128 v[112:115], v19 offset:20480\n\t"
    "v_lshl_add_u32 v88, v18, 4, v88\n\t"
    "v_mfma_f32_32x32x16_bf16 v[64:79], a[44:47], v[116:119], v[64:79]\n\t"
    "buffer_load_dwordx4 v19, s[16:19], s42 offen offset:2048 sc1 lds\n\t"
    "s_waitcnt lgkmcnt(5)\n\t"
    "v_mfma_f32_32x32x16_bf16 v[20:35], a[48:51], v[120:123], v[20:35]\n\t"
    "ds_read_b128 v[116:119], v19 offset:21504\n\t"
    "v_mov_b32_dpp v81, v80 row_ror:8 row_mask:0xf bank_mask:0xf\n\t"
    "v_mul_f32 v90, 0xbb000000, v80\n\t"
    "v_mfma_f32_32x32x16_bf16 v[64:79], a[52:55], v[120:123], v[64:79]\n\t"
    "buffer_load_dwordx4 v19, s[16:19], s42 offen offset:3072 sc1 lds\n\t"
    "v_mfma_f32_32x32x16_bf16 v[20:35], a[56:59], v[124:127], v[20:35]\n\t"
    "ds_read_b128 v[120:123], v19 offset:22528\n\t"
    "v_mul_f32 v81, 0x3b000000, v81\n\t"
    "v_fma_f32 v87, -v90, v90, v81\n\t"
    "v_mfma_f32_32x32x16_bf16 v[64:79], a[60:63], v[124:127], v[64:79]\n\t"
    "v_cvt_pk_bf16_f32 v84, v90, v90\n\t"
    "v_max_f32 v87, 0, v87\n\t"
    "s_waitcnt lgkmcnt(5)\n\t"
    "v_mfma_f32_32x32x16_bf16 v[20:35], a[96:99], v[128:131], v[20:35]\n\t"
    "ds_read_b128 v[124:127], v19 offset:23552\n\t"
    "s_add_u32 m0, s63, 131072\n\t"
    "s_add_u32 s42, s50, 0\n\t"
    "buffer_load_dwordx4 v41, s[8:11], s42 offen offset:0 lds\n\t"
    "v_mfma_f32_32x32x16_bf16 v[64:79], a[100:103], v[128:131], v[64:79]\n\t"
    "v_and_b32 v85, 0xffff0000, v84\n\t"
    "v_add_f32 v87, 0x3727c5ac, v87\n\t"
    "v_mfma_f32_32x32x16_bf16 v[20:35], a[104:107], v[132:135], v[20:35]\n\t"
    "ds_read_b128 v[128:131], v19 offset:28672\n\t"
    "buffer_load_dwordx4 v45, s[8:11], s42 offen offset:1024 lds\n\t"
    "v_mfma_f32_32x32x16_bf16 v[64:79], a[108:111], v[132:135], v[64:79]\n\t"
    "v_sub_f32 v85, v90, v85\n\t"
    "v_rsq_f32 v86, v87\n\t"
    "s_waitcnt lgkmcnt(5)\n\t"
    "v_mfma_f32_32x32x16_bf16 v[20:35], a[112:115], v[136:139], v[20:35]\n\t"
    "ds_read_b128 v[132:135], v19 offset:29696\n\t"
    "v_add_u32 v88, s63, v88\n\t"
    "v_mfma_f32_32x32x16_bf16 v[64:79], a[116:119], v[136:139], v[64:79]\n\t"
    "s_add_u32 m0, s63, 133120\n\t"
    "s_add_u32 s42, s50, 16384\n\t"
    "buffer_load_dwordx4 v41, s[8:11], s42 offen offset:0 lds\n\t"
    "v_mfma_f32_32x32x16_bf16 v[20:35], a[120:123], v[140:143], v[20:35]\n\t"
    "ds_read_b128 v[136:139], v19 offset:30720\n\t"
    "v_add_u32 v88, 137216, v88\n\t"
    "v_mul_f32 v86, 0xbfb8aa3b, v86\n\t"
    "v_mfma_f32_32x32x16_bf16 v[64:79], a[124:127], v[140:143], v[64:79]\n\t"
    "buffer_load_dwordx4 v45, s[8:11], s42 offen offset:1024 lds\n\t"
    "s_waitcnt lgkmcnt(5)\n\t"
    "v_mfma_f32_32x32x16_bf16 v[20:35], a[160:163], v[112:115], v[20:35]\n\t"
    "ds_read_b128 v[140:143], v19 offset:31744\n\t"
    "v_cvt_pk_bf16_f32 v85, v85, v85\n\t"
    "v_mfma_f32_32x32x16_bf16 v[64:79], a[164:167], v[112:115], v[64:79]\n\t"
    "v_and_b32 v87, 0xffffff7f, v87\n\t"
    "v_and_b32 v86, 0xffffff7f, v86\n\t"
    "ds_write_b128 v88, v[84:87]\n\t"
    "v_mfma_f32_32x32x16_bf16 v[20:35], a[168:171], v[116:119], v[20:35]\n\t"
    "s_add_u32 m0, s63, 135168\n\t"
    "s_add_u32 s42, s50, 32768\n\t"
    "buffer_load_dwordx4 v41, s[8:11], s42 offen offset:0 lds\n\t"
    "v_mfma_f32_32x32x16_bf16 v[64:79], a[172:175], v[116:119], v[64:79]\n\t"
    "s_waitcnt lgkmcnt(5)\n\t"
    "v_mfma_f32_32x32x16_bf16 v[20:35], a[176:179], v[120:123], v[20:35]\n\t"
    "buffer_load_dwordx4 v45, s[8:11], s42 offen offset:1024 lds\n\t"
    "v_mfma_f32_32x32x16_bf16 v[64:79], a[180:183], v[120:123], v[64:79]\n\t"
    "v_mfma_f32_32x32x16_bf16 v[20:35], a[184:187], v[124:127], v[20:35]\n\t"
    "v_mfma_f32_32x32x16_bf16 v[64:79], a[188:191], v[124:127], v[64:79]\n\t"
    "s_waitcnt lgkmcnt(3)\n\t"
    "v_mfma_f32_32x32x16_bf16 v[20:35], a[224:227], v[128:131], v[20:35]\n\t"
    "v_mfma_f32_32x32x16_bf16 v[64:79], a[228:231], v[128:131], v[64:79]\n\t"
    "v_mfma_f32_32x32x16_bf16 v[20:35], a[232:235], v[132:135], v[20:35]\n\t"
    "s_waitcnt lgkmcnt(0)\n\t"
    "s_waitcnt vmcnt(14)\n\t"
    "s_barrier\n\t"
    "ds_read_b128 v[112:115], v19 offset:32768\n\t"
    "s_add_u32 m0, s63, 0\n\t"
    "s_add_u32 s42, s50, 49152\n\t"
    "buffer_load_dwordx4 v41, s[8:11], s42 offen offset:0 lds\n\t"
    "v_mfma_f32_32x32x16_bf16 v[64:79], a[236:239], v[132:135], v[64:79]\n\t"
    "ds_read_b128 v[116:119], v19 offset:33792\n\t"
    "ds_read_b128 v[144:147], v44 offset:0\n\t"
    "v_mfma_f32_32x32x16_bf16 v[20:35], a[240:243], v[136:139], v[20:35]\n\t"
    "ds_read_b128 v[120:123], v19 offset:34816\n\t"
    "buffer_load_dwordx4 v45, s[8:11], s42 offen offset:1024 lds\n\t"
    "v_mfma_f32_32x32x16_bf16 v[64:79], a[244:247], v[136:139], v[64:79]\n\t"
    "ds_read_b128 v[124:127], v19 offset:35840\n\t"
    "v_mfma_f32_32x32x16_bf16 v[20:35], a[248:251], v[140:143], v[20:35]\n\t"
    "ds_read_b128 v[128:131], v19 offset:36864\n\t"
    "v_mfma_f32_32x32x16_bf16 v[64:79], a[252:255], v[140:143], v[64:79]\n\t"
    "ds_read_b128 v[132:135], v19 offset:37888\n\t"
    "s_add_u32 m0, s63, 2048\n\t"
    "s_add_u32 s42, s50, 65536\n\t"
    "buffer_load_dwordx4 v41, s[8:11], s42 offen offset:0 lds\n\t"
    "s_waitcnt lgkmcnt(4)\n\t"
    "v_mfma_f32_32x32x16_bf16 v[20:35], v[36:39], v[144:147], v[20:35]\n\t"
    "ds_read_b128 v[136:139], v19 offset:38912\n\t"
    "v_mfma_f32_32x32x16_bf16 v[64:79], v[46:49], v[144:147], v[64:79]\n\t"
    "ds_read_b128 v[140:143], v19 offset:39936\n\t"
    "buffer_load_dwordx4 v45, s[8:11], s42 offen offset:1024 lds\n\t"
    "v_mfma_f32_32x32x16_bf16 v[80:95], a[0:3], v[112:115], 0\n\t"
    "v_mfma_f32_32x32x16_bf16 v[96:111], a[4:7], v[112:115], 0\n\t"
    "v_mfma_f32_32x32x16_bf16 v[80:95], a[8:11], v[116:119], v[80:95]\n\t"
    "ds_read_b128 v[112:115], v19 offset:40960\n\t"
    "s_add_u32 m0, s63, 4096\n\t"
    "s_add_u32 s42, s50, 81920\n\t"
    "buffer_load_dwordx4 v41, s[8:11], s42 offen offset:0 lds\n\t"
    "v_mfma_f32_32x32x16_bf16 v[96:111], a[12:15], v[116:119], v[96:111]\n\t"
    "s_nop 1\n\t"
    "v_mul_f32 v64, v20, v64\n\t"
    "v_mul_f32 v65, v21, v65\n\t"
    "v_mul_f32 v20, v146, v20\n\t"
    "v_mul_f32 v21, v146, v21\n\t"
    "s_waitcnt lgkmcnt(5)\n\t"
    "v_mfma_f32_32x32x16_bf16 v[80:95], a[16:19], v[120:123], v[80:95]\n\t"
    "ds_read_b128 v[116:119], v19 offset:41984\n\t"
    "buffer_load_dwordx4 v45, s[8:11], s42 offen offset:1024 lds\n\t"
    "v_exp_f32 v20, v20\n\t"
    "v_mfma_f32_32x32x16_bf16 v[96:111], a[20:23], v[120:123], v[96:111]\n\t"
    "v_exp_f32 v21, v21\n\t"
    "v_fma_f32 v20, v147, v20, v147\n\t"
    "v_mfma_f32_32x32x16_bf16 v[80:95], a[24:27], v[124:127], v[80:95]\n\t"
    "ds_read_b128 v[120:123], v19 offset:43008\n\t"
    "v_fma_f32 v21, v147, v21, v147\n\t"
    "v_rcp_f32 v20, v20\n\t"
    "v_mfma_f32_32x32x16_bf16 v[96:111], a[28:31], v[124:127], v[96:111]\n\t"
    "s_add_u32 m0, s63, 6144\n\t"
    "s_add_u32 s42, s50, 98304\n\t"
    "buffer_load_dwordx4 v41, s[8:11], s42 offen offset:0 lds\n\t"
    "s_waitcnt lgkmcnt(5)\n\t"
    "v_mfma_f32_32x32x16_bf16 v[80:95], a[32:35], v[128:131], v[80:95]\n\t"
    "ds_read_b128 v[124:127], v19 offset:44032\n\t"
    "v_rcp_f32 v21, v21\n\t"
    "v_mul_f32 v64, v20, v64\n\t"
    "v_mul_f32 v65, v21, v65\n\t"
    "v_mfma_f32_32x32x16_bf16 v[96:111], a[36:39], v[128:131], v[96:111]\n\t"
    "buffer_load_dwordx4 v45, s[8:11], s42 offen offset:1024 lds\n\t"
    "v_mul_f32 v66, v22, v66\n\t"
    "v_mfma_f32_32x32x16_bf16 v[80:95], a[40:43], v[132:135], v[80:95]\n\t"
    "ds_read_b128 v[128:131], v19 offset:45056\n\t"
    "v_mul_f32 v67, v23, v67\n\t"
    "v_mul_f32 v22, v146, v22\n\t"
    "v_mul_f32 v23, v146, v23\n\t"
    "v_mfma_f32_32x32x16_bf16 v[96:111], a[44:47], v[132:135], v[96:111]\n\t"
    "v_exp_f32 v22, v22\n\t"
    "v_exp_f32 v23, v23\n\t"
    "v_cvt_pk_bf16_f32 v20, v64, v65\n\t"
    "v_fma_f32 v22, v147, v22, v147\n\t"
    "s_waitcnt lgkmcnt(5)\n\t"
    "v_mfma_f32_32x32x16_bf16 v[80:95], a[48:51], v[136:139], v[80:95]\n\t"
    "ds_read_b128 v[132:135], v19 offset:46080\n\t"
    "v_fma_f32 v23, v147, v23, v147\n\t"
    "v_rcp_f32 v22, v22\n\t"
    "v_mfma_f32_32x32x16_bf16 v[96:111], a[52:55], v[136:139], v[96:111]\n\t"
    "v_rcp_f32 v23, v23\n\t"
    "v_mul_f32 v66, v22, v66\n\t"
    "v_mul_f32 v67, v23, v67\n\t"
    "v_mfma_f32_32x32x16_bf16 v[80:95], a[56:59], v[140:143], v[80:95]\n\t"
    "ds_read_b128 v[136:139], v19 offset:47104\n\t"
    "s_waitcnt vmcnt(12)\n\t"
    "ds_read_b64_tr_b16 v[50:51], v40 offset:0\n\t"
    "v_mul_f32 v68, v24, v68\n\t"
    "v_mul_f32 v69, v25, v69\n\t"
    "v_mfma_f32_32x32x16_bf16 v[96:111], a[60:63], v[140:143], v[96:111]\n\t"
    "ds_read_b64_tr_b16 v[52:53], v40 offset:1024\n\t"
    "v_mul_f32 v24, v146, v24\n\t"
    "v_mul_f32 v25, v146, v25\n\t"
    "v_exp_f32 v24, v24\n\t"
    "s_waitcnt lgkmcnt(7)\n\t"
    "v_mfma_f32_32x32x16_bf16 v[80:95], a[64:67], v[112:115], v[80:95]\n\t"
    "ds_read_b128 v[140:143], v19 offset:48128\n\t"
    "ds_read_b64_tr_b16 v[54:55], v43 offset:0\n\t"
    "v_exp_f32 v25, v25\n\t"
    "v_mfma_f32_32x32x16_bf16 v[96:111], a[68:71], v[112:115], v[96:111]\n\t"
    "ds_read_b64_tr_b16 v[56:57], v43 offset:1024\n\t"
    "v_cvt_pk_bf16_f32 v21, v66, v67\n\t"
    "v_fma_f32 v24, v147, v24, v147\n\t"
    "v_fma_f32 v25, v147, v25, v147\n\t"
    "v_mfma_f32_32x32x16_bf16 v[80:95], a[72:75], v[116:119], v[80:95]\n\t"
    "ds_read_b128 v[112:115], v19 offset:49152\n\t"
    "v_rcp_f32 v24, v24\n\t"
    "v_rcp_f32 v25, v25\n\t"
    "v_mfma_f32_32x32x16_bf16 v[96:111], a[76:79], v[116:119], v[96:111]\n\t"
    "s_waitcnt vmcnt(10)\n\t"
    "ds_read_b64_tr_b16 v[58:59], v40 offset:2048\n\t"
    "v_mul_f32 v68, v24, v68\n\t"
    "v_mul_f32 v69, v25, v69\n\t"
    "s_waitcnt lgkmcnt(10)\n\t"
    "v_mfma_f32_32x32x16_bf16 v[80:95], a[80:83], v[120:123], v[80:95]\n\t"
    "ds_read_b128 v[116:119], v19 offset:50176\n\t"
    "ds_read_b64_tr_b16 v[60:61], v40 offset:3072\n\t"
    "v_mul_f32 v70, v26, v70\n\t"
    "v_mul_f32 v71, v27, v71\n\t"
    "v_mul_f32 v26, v146, v26\n\t"
    "v_mfma_f32_32x32x16_bf16 v[96:111], a[84:87], v[120:123], v[96:111]\n\t"
    "ds_read_b64_tr_b16 v[152:153], v43 offset:2048\n\t"
    "v_mul_f32 v27, v146, v27\n\t"
    "v_exp_f32 v26, v26\n\t"
    "v_mfma_f32_32x32x16_bf16 v[80:95], a[88:91], v[124:127], v[80:95]\n\t"
    "ds_read_b128 v[120:123], v19 offset:51200\n\t"
    "ds_read_b64_tr_b16 v[154:155], v43 offset:3072\n\t"
    "s_waitcnt lgkmcnt(7)\n\t"
    "s_add_u32 m0, s63, 131072\n\t"
    "s_add_u32 s42, s51, 0\n\t"
    "buffer_load_dwordx4 v41, s[8:11], s42 offen offset:0 lds\n\t"
    "v_mfma_f32_32x32x16_bf16 v[96:111], a[92:95], v[124:127], v[96:111]\n\t"
    "v_exp_f32 v27, v27\n\t"
    "v_cvt_pk_bf16_f32 v22, v68, v69\n\t"
    "v_fma_f32 v26, v147, v26, v147\n\t"
    "v_mfma_f32_32x32x16_bf16 v[80:95], a[96:99], v[128:131], v[80:95]\n\t"
    "ds_read_b128 v[124:127], v19 offset:52224\n\t"
    "s_waitcnt vmcnt(9)\n\t"
    "ds_read_b64_tr_b16 v[156:157], v40 offset:4096\n\t"
    "buffer_load_dwordx4 v45, s[8:11], s42 offen offset:1024 lds\n\t"
    "v_mfma_f32_32x32x16_bf16 v[96:111], a[100:103], v[128:131], v[96:111]\n\t"
    "ds_read_b64_tr_b16 v[158:159], v40 offset:5120\n\t"
    "v_fma_f32 v27, v147, v27, v147\n\t"
    "v_rcp_f32 v26, v26\n\t"
    "v_mfma_f32_32x32x16_bf16 v[80:95], a[104:107], v[132:135], v[80:95]\n\t"
    "ds_read_b128 v[128:131], v19 offset:53248\n\t"
    "ds_read_b64_tr_b16 v[160:161], v43 offset:4096\n\t"
    "v_rcp_f32 v27, v27\n\t"
    "v_mfma_f32_32x32x16_bf16 v[96:111], a[108:111], v[132:135], v[96:111]\n\t"
    "ds_read_b64_tr_b16 v[162:163], v43 offset:5120\n\t"
    "s_add_u32 m0, s63, 98304\n\t"
    "s_add_u32 s42, s44, 98304\n\t"
    "buffer_load_dwordx4 v19, s[16:19], s42 offen offset:0 sc1 lds\n\t"
    "v_mfma_f32_32x32x16_bf16 v[80:95], a[112:115], v[136:139], v[80:95]\n\t"
    "ds_read_b128 v[132:135], v19 offset:54272\n\t"
    "v_mul_f32 v70, v26, v70\n\t"
    "v_mul_f32 v71, v27, v71\n\t"
    "v_mul_f32 v72, v28, v72\n\t"
    "v_mul_f32 v73, v29, v73\n\t"
    "v_mfma_f32_32x32x16_bf16 v[96:111], a[116:119], v[136:139], v[96:111]\n\t"
    "buffer_load_dwordx4 v19, s[16:19], s42 offen offset:1024 sc1 lds\n\t"
    "v_mul_f32 v28, v146, v28\n\t"
    "v_mfma_f32_32x32x16_bf16 v[80:95], a[120:123], v[140:143], v[80:95]\n\t"
    "ds_read_b128 v[136:139], v19 offset:55296\n\t"
    "v_mul_f32 v29, v146, v29\n\t"
    "v_exp_f32 v28, v28\n\t"
    "v_mfma_f32_32x32x16_bf16 v[96:111], a[124:127], v[140:143], v[96:111]\n\t"
    "v_exp_f32 v29, v29\n\t"
    "v_cvt_pk_bf16_f32 v23, v70, v71\n\t"
    "v_fma_f32 v28, v147, v28, v147\n\t"
    "v_fma_f32 v29, v147, v29, v147\n\t"
    "s_waitcnt lgkmcnt(12)\n\t"
    "v_mfma_f32_32x32x16_bf16 v[80:95], a[128:131], v[112:115], v[80:95]\n\t"
    "ds_read_b128 v[140:143], v19 offset:56320\n\t"
    "s_waitcnt vmcnt(10)\n\t"
    "ds_read_b64_tr_b16 v[164:165], v6 offset:0\n\t"
    "buffer_load_dwordx4 v19, s[16:19], s42 offen offset:2048 sc1 lds\n\t"
    "v_mfma_f32_32x32x16_bf16 v[96:111], a[132:135], v[112:115], v[96:111]\n\t"
    "ds_read_b64_tr_b16 v[166:167], v6 offset:1024\n\t"
    "v_rcp_f32 v28, v28\n\t"
    "v_mfma_f32_32x32x16_bf16 v[80:95], a[136:139], v[116:119], v[80:95]\n\t"
    "ds_read_b128 v[112:115], v19 offset:57344\n\t"
    "ds_read_b64_tr_b16 v[168:169], v13 offset:0\n\t"
    "buffer_load_dwordx4 v19, s[16:19], s42 offen offset:3072 sc1 lds\n\t"
    "v_mfma_f32_32x32x16_bf16 v[96:111], a[140:143], v[116:119], v[96:111]\n\t"
    "ds_read_b64_tr_b16 v[170:171], v13 offset:1024\n\t"
    "v_rcp_f32 v29, v29\n\t"
    "s_add_u32 s39, s45, 0\n\t"
    "buffer_store_dwordx4 v[20:23], v19, s[16:19], s39 offen offset:0 sc1\n\t"
    "s_waitcnt lgkmcnt(13)\n\t"
    "v_mfma_f32_32x32x16_bf16 v[80:95], a[144:147], v[120:123], v[80:95]\n\t"
    "ds_read_b128 v[116:119], v19 offset:58368\n\t"
    "v_mul_f32 v72, v28, v72\n\t"
    "v_mul_f32 v73, v29, v73\n\t"
    "v_mfma_f32_32x32x16_bf16 v[96:111], a[148:151], v[120:123], v[96:111]\n\t"
    "s_waitcnt vmcnt(11)\n\t"
    "ds_read_b64_tr_b16 v[172:173], v6 offset:2048\n\t"
    "s_add_u32 m0, s63, 102400\n\t"
    "s_add_u32 s42, s46, 98304\n\t"
    "buffer_load_dwordx4 v19, s[16:19], s42 offen offset:0 sc1 lds\n\t"
    "v_mfma_f32_32x32x16_bf16 v[80:95], a[152:155], v[124:127], v[80:95]\n\t"
    "ds_read_b128 v[120:123], v19 offset:59392\n\t"
    "ds_read_b64_tr_b16 v[174:175], v6 offset:3072\n\t"
    "v_mul_f32 v74, v30, v74\n\t"
    "v_mul_f32 v75, v31, v75\n\t"
    "v_mul_f32 v30, v146, v30\n\t"
    "v_mfma_f32_32x32x16_bf16 v[96:111], a[156:159], v[124:127], v[96:111]\n\t"
    "ds_read_b64_tr_b16 v[176:177], v13 offset:2048\n\t"
    "buffer_load_dwordx4 v19, s[16:19], s42 offen offset:1024 sc1 lds\n\t"
    "v_mul_f32 v31, v146, v31\n\t"
    "s_waitcnt lgkmcnt(12)\n\t"
    "v_mfma_f32_32x32x16_bf16 v[80:95], a[160:163], v[128:131], v[80:95]\n\t"
    "ds_read_b128 v[124:127], v19 offset:60416\n\t"
    "ds_read_b64_tr_b16 v[178:179], v13 offset:3072\n\t"
    "v_exp_f32 v30, v30\n\t"
    "v_mfma_f32_32x32x16_bf16 v[96:111], a[164:167], v[128:131], v[96:111]\n\t"
    "v_exp_f32 v31, v31\n\t"
    "v_cvt_pk_bf16_f32 v24, v72, v73\n\t"
    "v_fma_f32 v30, v147, v30, v147\n\t"
    "v_mfma_f32_32x32x16_bf16 v[80:95], a[168:171], v[132:135], v[80:95]\n\t"
    "ds_read_b128 v[128:131], v19 offset:61440\n\t"
    "s_waitcnt vmcnt(11)\n\t"
    "ds_read_b64_tr_b16 v[180:181], v6 offset:4096\n\t"
    "buffer_load_dwordx4 v19, s[16:19], s42 offen offset:2048 sc1 lds\n\t"
    "v_mfma_f32_32x32x16_bf16 v[96:111], a[172:175], v[132:135], v[96:111]\n\t"
    "ds_read_b64_tr_b16 v[182:183], v6 offset:5120\n\t"
    "v_fma_f32 v31, v147, v31, v147\n\t"
    "v_rcp_f32 v30, v30\n\t"
    "s_waitcnt lgkmcnt(14)\n\t"
    "v_mfma_f32_32x32x16_bf16 v[80:95], a[176:179], v[136:139], v[80:95]\n\t"
    "ds_read_b128 v[132:135], v19 offset:62464\n\t"
    "ds_read_b64_tr_b16 v[184:185], v13 offset:4096\n\t"
    "buffer_load_dwordx4 v19, s[16:19], s42 offen offset:3072 sc1 lds\n\t"
    "v_mfma_f32_32x32x16_bf16 v[96:111], a[180:183], v[136:139], v[96:111]\n\t"
    "ds_read_b64_tr_b16 v[186:187], v13 offset:5120\n\t"
    "v_rcp_f32 v31, v31\n\t"
    "v_mul_f32 v74, v30, v74\n\t"
    "v_mfma_f32_32x32x16_bf16 v[80:95], a[184:187], v[140:143], v[80:95]\n\t"
    "ds_read_b128 v[136:139], v19 offset:63488\n\t"
    "v_mul_f32 v75, v31, v75\n\t"
    "v_mul_f32 v76, v32, v76\n\t"
    "v_mul_f32 v77, v33, v77\n\t"
    "v_mul_f32 v32, v146, v32\n\t"
    "v_mfma_f32_32x32x16_bf16 v[96:111], a[188:191], v[140:143], v[96:111]\n\t"
    "s_waitcnt vmcnt(11)\n\t"
    "ds_read_b64_tr_b16 v[188:189], v6 offset:6144\n\t"
    "s_add_u32 m0, s63, 133120\n\t"
    "s_add_u32 s42, s51, 16384\n\t"
    "buffer_load_dwordx4 v41, s[8:11], s42 offen offset:0 lds\n\t"
    "s_waitcnt lgkmcnt(14)\n\t"
    "v_mfma_f32_32x32x16_bf16 v[80:95], a[192:195], v[112:115], v[80:95]\n\t"
    "ds_read_b128 v[140:143], v19 offset:64512\n\t"
    "ds_read_b64_tr_b16 v[190:191], v6 offset:7168\n\t"
    "v_mul_f32 v33, v146, v33\n\t"
    "v_exp_f32 v32, v32\n\t"
    "v_mfma_f32_32x32x16_bf16 v[96:111], a[196:199], v[112:115], v[96:111]\n\t"
    "ds_read_b64_tr_b16 v[192:193], v13 offset:6144\n\t"
    "buffer_load_dwordx4 v45, s[8:11], s42 offen offset:1024 lds\n\t"
    "v_mfma_f32_32x32x16_bf16 v[80:95], a[200:203], v[116:119], v[80:95]\n\t"
    "ds_read_b64_tr_b16 v[194:195], v13 offset:7168\n\t"
    "v_exp_f32 v33, v33\n\t"
    "v_cvt_pk_bf16_f32 v25, v74, v75\n\t"
    "v_mfma_f32_32x32x16_bf16 v[96:111], a[204:207], v[116:119], v[96:111]\n\t"
    "v_fma_f32 v32, v147, v32, v147\n\t"
    "v_fma_f32 v33, v147, v33, v147\n\t"
    "v_rcp_f32 v32, v32\n\t"
    "v_rcp_f32 v33, v33\n\t"
    "s_waitcnt lgkmcnt(13)\n\t"
    "v_mfma_f32_32x32x16_bf16 v[80:95], a[208:211], v[120:123], v[80:95]\n\t"
    "s_add_u32 m0, s63, 135168\n\t"
    "s_add_u32 s42, s51, 32768\n\t"
    "buffer_load_dwordx4 v41, s[8:11], s42 offen offset:0 lds\n\t"
    "v_mfma_f32_32x32x16_bf16 v[96:111], a[212:215], v[120:123], v[96:111]\n\t"
    "v_mul_f32 v76, v32, v76\n\t"
    "v_mul_f32 v77, v33, v77\n\t"
    "v_mul_f32 v78, v34, v78\n\t"
    "v_mfma_f32_32x32x16_bf16 v[80:95], a[216:219], v[124:127], v[80:95]\n\t"
    "buffer_load_dwordx4 v45, s[8:11], s42 offen offset:1024 lds\n\t"
    "v_mul_f32 v79, v35, v79\n\t"
    "v_mul_f32 v34, v146, v34\n\t"
    "v_mfma_f32_32x32x16_bf16 v[96:111], a[220:223], v[124:127], v[96:111]\n\t"
    "v_mul_f32 v35, v146, v35\n\t"
    "v_exp_f32 v34, v34\n\t"
    "v_exp_f32 v35, v35\n\t"
    "s_waitcnt lgkmcnt(8)\n\t"
    "v_mfma_f32_32x32x16_bf16 v[80:95], a[224:227], v[128:131], v[80:95]\n\t"
    "v_cvt_pk_bf16_f32 v26, v76, v77\n\t"
    "v_fma_f32 v34, v147, v34, v147\n\t"
    "v_fma_f32 v35, v147, v35, v147\n\t"
    "v_mfma_f32_32x32x16_bf16 v[96:111], a[228:231], v[128:131], v[96:111]\n\t"
    "s_add_u32 m0, s63, 0\n\t"
    "s_add_u32 s42, s51, 49152\n\t"
    "buffer_load_dwordx4 v41, s[8:11], s42 offen offset:0 lds\n\t"
    "v_mfma_f32_32x32x16_bf16 v[80:95], a[232:235], v[132:135], v[80:95]\n\t"
    "s_waitcnt lgkmcnt(3)\n\t"
    "s_barrier\n\t"
    "ds_read_b128 v[112:115], v5 offset:0\n\t"
    "v_rcp_f32 v34, v34\n\t"
    "v_rcp_f32 v35, v35\n\t"
    "v_mul_f32 v78, v34, v78\n\t"
    "v_mfma_f32_32x32x16_bf16 v[96:111], a[236:239], v[132:135], v[96:111]\n\t"
    "ds_read_b128 v[116:119], v5 offset:1024\n\t"
    "ds_read_b128 v[148:151], v44 offset:8192\n\t"
    "buffer_load_dwordx4 v45, s[8:11], s42 offen offset:1024 lds\n\t"
    "v_mfma_f32_32x32x16_bf16 v[80:95], a[240:243], v[136:139], v[80:95]\n\t"
    "ds_read_b128 v[120:123], v5 offset:2048\n\t"
    "v_mul_f32 v79, v35, v79\n\t"
    "v_cvt_pk_bf16_f32 v27, v78, v79\n\t"
    "v_mfma_f32_32x32x16_bf16 v[96:111], a[244:247], v[136:139], v[96:111]\n\t"
    "ds_read_b128 v[124:127], v5 offset:3072\n\t"
    "buffer_store_dwordx4 v[24:27], v19, s[16:19], s39 offen offset:1024 sc1\n\t"
    "v_mfma_f32_32x32x16_bf16 v[80:95], a[248:251], v[140:143], v[80:95]\n\t"
    "ds_read_b128 v[128:131], v5 offset:4096\n\t"
    "s_waitcnt vmcnt(16)\n\t"
    "ds_read_b64_tr_b16 v[196:197], v40 offset:0\n\t"
    "s_add_u32 m0, s63, 2048\n\t"
    "s_add_u32 s42, s51, 65536\n\t"
    "buffer_load_dwordx4 v41, s[8:11], s42 offen offset:0 lds\n\t"
    "v_mfma_f32_32x32x16_bf16 v[96:111], a[252:255], v[140:143], v[96:111]\n\t"
    "ds_read_b128 v[132:135], v5 offset:5120\n\t"
    "ds_read_b64_tr_b16 v[198:199], v40 offset:1024\n\t"
    "s_waitcnt lgkmcnt(6)\n\t"
    "v_mfma_f32_32x32x16_bf16 v[80:95], v[36:39], v[148:151], v[80:95]\n\t"
    "ds_read_b128 v[136:139], v5 offset:6144\n\t"
    "ds_read_b64_tr_b16 v[200:201], v43 offset:0\n\t"
    "buffer_load_dwordx4 v45, s[8:11], s42 offen offset:1024 lds\n\t"
    "v_mfma_f32_32x32x16_bf16 v[96:111], v[46:49], v[148:151], v[96:111]\n\t"
    "ds_read_b128 v[140:143], v5 offset:7168\n\t"
    "ds_read_b64_tr_b16 v[202:203], v43 offset:1024\n\t"
    "v_mfma_f32_32x32x16_bf16 v[20:35], a[0:3], v[112:115], 0\n\t"
    "v_mfma_f32_32x32x16_bf16 v[64:79], a[4:7], v[112:115], 0\n\t"
    "s_add_u32 m0, s63, 4096\n\t"
    "s_add_u32 s42, s51, 81920\n\t"
    "buffer_load_dwordx4 v41, s[8:11], s42 offen offset:0 lds\n\t"
    "v_mfma_f32_32x32x16_bf16 v[20:35], a[8:11], v[116:119], v[20:35]\n\t"
    "ds_read_b128 v[112:115], v5 offset:8192\n\t"
    "s_nop 2\n\t"
    "v_mul_f32 v96, v80, v96\n\t"
    "v_mul_f32 v97, v81, v97\n\t"
    "v_mul_f32 v80, v150, v80\n\t"
    "v_mul_f32 v81, v150, v81\n\t"
    "v_exp_f32 v80, v80\n\t"
    "v_mfma_f32_32x32x16_bf16 v[64:79], a[12:15], v[116:119], v[64:79]\n\t"
    "buffer_load_dwordx4 v45, s[8:11], s42 offen offset:1024 lds\n\t"
    "v_exp_f32 v81, v81\n\t"
    "s_waitcnt lgkmcnt(9)\n\t"
    "v_mfma_f32_32x32x16_bf16 v[20:35], a[16:19], v[120:123], v[20:35]\n\t"
    "ds_read_b128 v[116:119], v5 offset:9216\n\t"
    "v_fma_f32 v80, v151, v80, v151\n\t"
    "v_fma_f32 v81, v151, v81, v151\n\t"
    "v_rcp_f32 v80, v80\n\t"
    "v_mfma_f32_32x32x16_bf16 v[64:79], a[20:23], v[120:123], v[64:79]\n\t"
    "v_rcp_f32 v81, v81\n\t"
    "v_mul_f32 v96, v80, v96\n\t"
    "v_mul_f32 v97, v81, v97\n\t"
    "v_mul_f32 v98, v82, v98\n\t"
    "v_mul_f32 v99, v83, v99\n\t"
    "v_mfma_f32_32x32x16_bf16 v[20:35], a[24:27], v[124:127], v[20:35]\n\t"
    "ds_read_b128 v[120:123], v5 offset:10240\n\t"
    "s_add_u32 m0, s63, 6144\n\t"
    "s_add_u32 s42, s51, 98304\n\t"
    "buffer_load_dwordx4 v41, s[8:11], s42 offen offset:0 lds\n\t"
    "v_mfma_f32_32x32x16_bf16 v[64:79], a[28:31], v[124:127], v[64:79]\n\t"
    "v_mul_f32 v82, v150, v82\n\t"
    "v_mul_f32 v83, v150, v83\n\t"
    "v_exp_f32 v82, v82\n\t"
    "v_exp_f32 v83, v83\n\t"
    "s_waitcnt lgkmcnt(8)\n\t"
    "v_mfma_f32_32x32x16_bf16 v[20:35], a[32:35], v[128:131], v[20:35]\n\t"
    "ds_read_b128 v[124:127], v5 offset:11264\n\t"
    "buffer_load_dwordx4 v45, s[8:11], s42 offen offset:1024 lds\n\t"
    "v_cvt_pk_bf16_f32 v80, v96, v97\n\t"
    "v_mfma_f32_32x32x16_bf16 v[64:79], a[36:39], v[128:131], v[64:79]\n\t"
    "v_fma_f32 v82, v151, v82, v151\n\t"
    "v_fma_f32 v83, v151, v83, v151\n\t"
    "v_rcp_f32 v82, v82\n\t"
    "v_rcp_f32 v83, v83\n\t"
    "v_mfma_f32_32x32x16_bf16 v[20:35], a[40:43], v[132:135], v[20:35]\n\t"
    "ds_read_b128 v[128:131], v5 offset:12288\n\t"
    "v_mul_f32 v98, v82, v98\n\t"
    "v_mul_f32 v99, v83, v99\n\t"
    "v_mul_f32 v100, v84, v100\n\t"
    "v_mul_f32 v101, v85, v101\n\t"
    "v_mul_f32 v84, v150, v84\n\t"
    "v_mfma_f32_32x32x16_bf16 v[64:79], a[44:47], v[132:135], v[64:79]\n\t"
    "s_add_u32 m0, s63, 32768\n\t"
    "s_add_u32 s42, s52, 0\n\t"
    "buffer_load_dwordx4 v41, s[8:11], s42 offen offset:0 lds\n\t"
    "s_waitcnt lgkmcnt(6)\n\t"
    "v_mfma_f32_32x32x16_bf16 v[20:35], a[48:51], v[136:139], v[20:35]\n\t"
    "ds_read_b128 v[132:135], v5 offset:13312\n\t"
    "v_mul_f32 v85, v150, v85\n\t"
    "v_exp_f32 v84, v84\n\t"
    "v_exp_f32 v85, v85\n\t"
    "v_mfma_f32_32x32x16_bf16 v[64:79], a[52:55], v[136:139], v[64:79]\n\t"
    "buffer_load_dwordx4 v45, s[8:11], s42 offen offset:1024 lds\n\t"
    "v_cvt_pk_bf16_f32 v81, v98, v99\n\t"
    "v_fma_f32 v84, v151, v84, v151\n\t"
    "v_fma_f32 v85, v151, v85, v151\n\t"
    "v_mfma_f32_32x32x16_bf16 v[20:35], a[56:59], v[140:143], v[20:35]\n\t"
    "ds_read_b128 v[136:139], v5 offset:14336\n\t"
    "v_rcp_f32 v84, v84\n\t"
    "v_rcp_f32 v85, v85\n\t"
    "v_mul_f32 v100, v84, v100\n\t"
    "v_mfma_f32_32x32x16_bf16 v[64:79], a[60:63], v[140:143], v[64:79]\n\t"
    "v_mul_f32 v101, v85, v101\n\t"
    "v_mul_f32 v102, v86, v102\n\t"
    "v_mul_f32 v103, v87, v103\n\t"
    "v_mul_f32 v86, v150, v86\n\t"
    "v_mul_f32 v87, v150, v87\n\t"
    "s_waitcnt lgkmcnt(5)\n\t"
    "v_mfma_f32_32x32x16_bf16 v[20:35], a[64:67], v[112:115], v[20:35]\n\t"
    "ds_read_b128 v[140:143], v5 offset:15360\n\t"
    "s_add_u32 m0, s63, 34816\n\t"
    "s_add_u32 s42, s52, 16384\n\t"
    "buffer_load_dwordx4 v41, s[8:11], s42 offen offset:0 lds\n\t"
    "v_mfma_f32_32x32x16_bf16 v[64:79], a[68:71], v[112:115], v[64:79]\n\t"
    "v_exp_f32 v86, v86\n\t"
    "v_exp_f32 v87, v87\n\t"
    "v_cvt_pk_bf16_f32 v82, v100, v101\n\t"
    "v_fma_f32 v86, v151, v86, v151\n\t"
    "v_mfma_f32_32x32x16_bf16 v[20:35], a[72:75], v[116:119], v[20:35]\n\t"
    "ds_read_b128 v[112:115], v5 offset:16384\n\t"
    "buffer_load_dwordx4 v45, s[8:11], s42 offen offset:1024 lds\n\t"
    "v_fma_f32 v87, v151, v87, v151\n\t"
    "v_rcp_f32 v86, v86\n\t"
    "v_mfma_f32_32x32x16_bf16 v[64:79], a[76:79], v[116:119], v[64:79]\n\t"
    "v_rcp_f32 v87, v87\n\t"
    "v_mul_f32 v102, v86, v102\n\t"
    "v_mul_f32 v103, v87, v103\n\t"
    "s_waitcnt lgkmcnt(5)\n\t"
    "v_mfma_f32_32x32x16_bf16 v[20:35], a[80:83], v[120:123], v[20:35]\n\t"
    "ds_read_b128 v[116:119], v5 offset:17408\n\t"
    "v_mul_f32 v104, v88, v104\n\t"
    "v_mul_f32 v105, v89, v105\n\t"
    "v_mul_f32 v88, v150, v88\n\t"
    "v_mul_f32 v89, v150, v89\n\t"
    "v_exp_f32 v88, v88\n\t"
    "v_mfma_f32_32x32x16_bf16 v[64:79], a[84:87], v[120:123], v[64:79]\n\t"
    "s_waitcnt vmcnt(15)\n\t"
    "ds_read_b64_tr_b16 v[204:205], v40 offset:2048\n\t"
    "v_exp_f32 v89, v89\n\t"
    "v_cvt_pk_bf16_f32 v83, v102, v103\n\t"
    "v_mfma_f32_32x32x16_bf16 v[20:35], a[88:91], v[124:127], v[20:35]\n\t"
    "ds_read_b128 v[120:123], v5 offset:18432\n\t"
    "ds_read_b64_tr_b16 v[206:207], v40 offset:3072\n\t"
    "v_fma_f32 v88, v151, v88, v151\n\t"
    "v_fma_f32 v89, v151, v89, v151\n\t"
    "v_rcp_f32 v88, v88\n\t"
    "v_mfma_f32_32x32x16_bf16 v[64:79], a[92:95], v[124:127], v[64:79]\n\t"
    "ds_read_b64_tr_b16 v[208:209], v43 offset:2048\n\t"
    "v_rcp_f32 v89, v89\n\t"
    "s_add_u32 s39, s45, 114688\n\t"
    "s_waitcnt lgkmcnt(8)\n\t"
    "v_mfma_f32_32x32x16_bf16 v[20:35], a[96:99], v[128:131], v[20:35]\n\t"
    "ds_read_b128 v[124:127], v5 offset:19456\n\t"
    "ds_read_b64_tr_b16 v[210:211], v43 offset:3072\n\t"
    "buffer_store_dwordx4 v[80:83], v19, s[16:19], s39 offen offset:0 sc1\n\t"
    "v_mul_f32 v104, v88, v104\n\t"
    "v_mul_f32 v105, v89, v105\n\t"
    "v_mfma_f32_32x32x16_bf16 v[64:79], a[100:103], v[128:131], v[64:79]\n\t"
    "v_mul_f32 v106, v90, v106\n\t"
    "v_mul_f32 v107, v91, v107\n\t"
    "v_mul_f32 v90, v150, v90\n\t"
    "v_mul_f32 v91, v150, v91\n\t"
    "v_exp_f32 v90, v90\n\t"
    "v_mfma_f32_32x32x16_bf16 v[20:35], a[104:107], v[132:135], v[20:35]\n\t"
    "ds_read_b128 v[128:131], v5 offset:20480\n\t"
    "s_waitcnt vmcnt(14)\n\t"
    "ds_read_b64_tr_b16 v[212:213], v40 offset:4096\n\t"
    "v_exp_f32 v91, v91\n\t"
    "v_cvt_pk_bf16_f32 v84, v104, v105\n\t"
    "v_mfma_f32_32x32x16_bf16 v[64:79], a[108:111], v[132:135], v[64:79]\n\t"
    "ds_read_b64_tr_b16 v[214:215], v40 offset:5120\n\t"
    "v_fma_f32 v90, v151, v90, v151\n\t"
    "v_fma_f32 v91, v151, v91, v151\n\t"
    "v_rcp_f32 v90, v90\n\t"
    "s_waitcnt lgkmcnt(11)\n\t"
    "v_mfma_f32_32x32x16_bf16 v[20:35], a[112:115], v[136:139], v[20:35]\n\t"
    "ds_read_b128 v[132:135], v5 offset:21504\n\t"
    "ds_read_b64_tr_b16 v[216:217], v43 offset:4096\n\t"
    "v_rcp_f32 v91, v91\n\t"
    "v_mul_f32 v106, v90, v106\n\t"
    "v_mul_f32 v107, v91, v107\n\t"
    "v_mfma_f32_32x32x16_bf16 v[64:79], a[116:119], v[136:139], v[64:79]\n\t"
    "ds_read_b64_tr_b16 v[218:219], v43 offset:5120\n\t"
    "v_mul_f32 v108, v92, v108\n\t"
    "v_mul_f32 v109, v93, v109\n\t"
    "v_mul_f32 v92, v150, v92\n\t"
    "v_mul_f32 v93, v150, v93\n\t"
    "v_exp_f32 v92, v92\n\t"
    "v_mfma_f32_32x32x16_bf16 v[20:35], a[120:123], v[140:143], v[20:35]\n\t"
    "ds_read_b128 v[136:139], v5 offset:22528\n\t"
    "v_exp_f32 v93, v93\n\t"
    "v_cvt_pk_bf16_f32 v85, v106, v107\n\t"
    "v_fma_f32 v92, v151, v92, v151\n\t"
    "v_mfma_f32_32x32x16_bf16 v[64:79], a[124:127], v[140:143], v[64:79]\n\t"
    "s_waitcnt vmcnt(12)\n\t"
    "ds_read_b64_tr_b16 v[220:221], v6 offset:0\n\t"
    "v_fma_f32 v93, v151, v93, v151\n\t"
    "v_rcp_f32 v92, v92\n\t"
    "s_waitcnt lgkmcnt(14)\n\t"
    "v_mfma_f32_32x32x16_bf16 v[20:35], a[128:131], v[112:115], v[20:35]\n\t"
    "ds_read_b128 v[140:143], v5 offset:23552\n\t"
    "ds_read_b64_tr_b16 v[222:223], v6 offset:1024\n\t"
    "v_rcp_f32 v93, v93\n\t"
    "v_mul_f32 v108, v92, v108\n\t"
    "v_mul_f32 v109, v93, v109\n\t"
    "v_mfma_f32_32x32x16_bf16 v[64:79], a[132:135], v[112:115], v[64:79]\n\t"
    "ds_read_b64_tr_b16 v[224:225], v13 offset:0\n\t"
    "v_mul_f32 v110, v94, v110\n\t"
    "v_mul_f32 v111, v95, v111\n\t"
    "v_mul_f32 v94, v150, v94\n\t"
    "v_mul_f32 v95, v150, v95\n\t"
    "v_exp_f32 v94, v94\n\t"
    "v_mfma_f32_32x32x16_bf16 v[20:35], a[136:139], v[116:119], v[20:35]\n\t"
    "ds_read_b128 v[112:115], v5 offset:24576\n\t"
    "ds_read_b64_tr_b16 v[226:227], v13 offset:1024\n\t"
    "v_exp_f32 v95, v95\n\t"
    "v_cvt_pk_bf16_f32 v86, v108, v109\n\t"
    "v_mfma_f32_32x32x16_bf16 v[64:79], a[140:143], v[116:119], v[64:79]\n\t"
    "v_fma_f32 v94, v151, v94, v151\n\t"
    "v_fma_f32 v95, v151, v95, v151\n\t"
    "v_rcp_f32 v94, v94\n\t"
    "v_rcp_f32 v95, v95\n\t"
    "s_waitcnt lgkmcnt(14)\n\t"
    "v_mfma_f32_32x32x16_bf16 v[20:35], a[144:147], v[120:123], v[20:35]\n\t"
    "ds_read_b128 v[116:119], v5 offset:25600\n\t"
    "s_waitcnt vmcnt(9)\n\t"
    "ds_read_b64_tr_b16 v[228:229], v6 offset:2048\n\t"
    "v_mul_f32 v110, v94, v110\n\t"
    "v_mul_f32 v111, v95, v111\n\t"
    "v_mfma_f32_32x32x16_bf16 v[64:79], a[148:151], v[120:123], v[64:79]\n\t"
    "ds_read_b64_tr_b16 v[230:231], v6 offset:3072\n\t"
    "v_cvt_pk_bf16_f32 v87, v110, v111\n\t"
    "buffer_store_dwordx4 v[84:87], v19, s[16:19], s39 offen offset:1024 sc1\n\t"
    "v_mfma_f32_32x32x16_bf16 v[20:35], a[152:155], v[124:127], v[20:35]\n\t"
    "ds_read_b128 v[120:123], v5 offset:26624\n\t"
    "ds_read_b64_tr_b16 v[232:233], v13 offset:2048\n\t"
    "v_mfma_f32_32x32x16_bf16 v[64:79], a[156:159], v[124:127], v[64:79]\n\t"
    "ds_read_b64_tr_b16 v[234:235], v13 offset:3072\n\t"
    "s_add_u32 m0, s63, 36864\n\t"
    "s_add_u32 s42, s52, 32768\n\t"
    "buffer_load_dwordx4 v41, s[8:11], s42 offen offset:0 lds\n\t"
    "s_waitcnt lgkmcnt(14)\n\t"
    "v_mfma_f32_32x32x16_bf16 v[20:35], a[160:163], v[128:131], v[20:35]\n\t"
    "ds_read_b128 v[124:127], v5 offset:27648\n\t"
    "buffer_load_dwordx4 v45, s[8:11], s42 offen offset:1024 lds\n\t"
    "v_mfma_f32_32x32x16_bf16 v[64:79], a[164:167], v[128:131], v[64:79]\n\t"
    "s_waitcnt vmcnt(10)\n\t"
    "ds_read_b64_tr_b16 v[236:237], v6 offset:4096\n\t"
    "v_mfma_f32_32x32x16_bf16 v[20:35], a[168:171], v[132:135], v[20:35]\n\t"
    "ds_read_b128 v[128:131], v5 offset:28672\n\t"
    "ds_read_b64_tr_b16 v[238:239], v6 offset:5120\n\t"
    "s_add_u32 m0, s63, 38912\n\t"
    "s_add_u32 s42, s52, 49152\n\t"
    "buffer_load_dwordx4 v41, s[8:11], s42 offen offset:0 lds\n\t"
    "v_mfma_f32_32x32x16_bf16 v[64:79], a[172:175], v[132:135], v[64:79]\n\t"
    "ds_read_b64_tr_b16 v[240:241], v13 offset:4096\n\t"
    "buffer_load_dwordx4 v45, s[8:11], s42 offen offset:1024 lds\n\t"
    "s_waitcnt lgkmcnt(14)\n\t"
    "v_mfma_f32_32x32x16_bf16 v[20:35], a[176:179], v[136:139], v[20:35]\n\t"
    "ds_read_b128 v[132:135], v5 offset:29696\n\t"
    "ds_read_b64_tr_b16 v[242:243], v13 offset:5120\n\t"
    "v_mfma_f32_32x32x16_bf16 v[64:79], a[180:183], v[136:139], v[64:79]\n\t"
    "s_add_u32 m0, s63, 131072\n\t"
    "s_add_u32 s42, s52, 65536\n\t"
    "buffer_load_dwordx4 v41, s[8:11], s42 offen offset:0 lds\n\t"
    "v_mfma_f32_32x32x16_bf16 v[20:35], a[184:187], v[140:143], v[20:35]\n\t"
    "ds_read_b128 v[136:139], v5 offset:30720\n\t"
    "buffer_load_dwordx4 v45, s[8:11], s42 offen offset:1024 lds\n\t"
    "v_mfma_f32_32x32x16_bf16 v[64:79], a[188:191], v[140:143], v[64:79]\n\t"
    "s_waitcnt lgkmcnt(13)\n\t"
    "v_mfma_f32_32x32x16_bf16 v[20:35], a[192:195], v[112:115], v[20:35]\n\t"
    "ds_read_b128 v[140:143], v5 offset:31744\n\t"
    "s_add_u32 m0, s63, 133120\n\t"
    "s_add_u32 s42, s52, 81920\n\t"
    "buffer_load_dwordx4 v41, s[8:11], s42 offen offset:0 lds\n\t"
    "v_mfma_f32_32x32x16_bf16 v[64:79], a[196:199], v[112:115], v[64:79]\n\t"
    "buffer_load_dwordx4 v45, s[8:11], s42 offen offset:1024 lds\n\t"
    "v_mfma_f32_32x32x16_bf16 v[20:35], a[200:203], v[116:119], v[20:35]\n\t"
    "v_mfma_f32_32x32x16_bf16 v[64:79], a[204:207], v[116:119], v[64:79]\n\t"
    "s_add_u32 m0, s63, 135168\n\t"
    "s_add_u32 s42, s52, 98304\n\t"
    "buffer_load_dwordx4 v41, s[8:11], s42 offen offset:0 lds\n\t"
    "s_waitcnt lgkmcnt(8)\n\t"
    "v_mfma_f32_32x32x16_bf16 v[20:35], a[208:211], v[120:123], v[20:35]\n\t"
    "buffer_load_dwordx4 v45, s[8:11], s42 offen offset:1024 lds\n\t"
    "v_mfma_f32_32x32x16_bf16 v[64:79], a[212:215], v[120:123], v[64:79]\n\t"
    "v_mfma_f32_32x32x16_bf16 v[20:35], a[216:219], v[124:127], v[20:35]\n\t"
    "s_add_u32 m0, s63, 0\n\t"
    "s_add_u32 s42, s53, 0\n\t"
    "buffer_load_dwordx4 v41, s[8:11], s42 offen offset:0 lds\n\t"
    "v_mfma_f32_32x32x16_bf16 v[64:79], a[220:223], v[124:127], v[64:79]\n\t"
    "buffer_load_dwordx4 v45, s[8:11], s42 offen offset:1024 lds\n\t"
    "s_waitcnt lgkmcnt(3)\n\t"
    "v_mfma_f32_32x32x16_bf16 v[20:35], a[224:227], v[128:131], v[20:35]\n\t"
    "v_mfma_f32_32x32x16_bf16 v[64:79], a[228:231], v[128:131], v[64:79]\n\t"
    "s_add_u32 m0, s63, 2048\n\t"
    "s_add_u32 s42, s53, 16384\n\t"
    "buffer_load_dwordx4 v41, s[8:11], s42 offen offset:0 lds\n\t"
    "v_mfma_f32_32x32x16_bf16 v[20:35], a[232:235], v[132:135], v[20:35]\n\t"
    "s_waitcnt lgkmcnt(0)\n\t"
    "s_waitcnt vmcnt(13)\n\t"
    "s_barrier\n\t"
    "s_cmp_lg_u32 s33, 1\n\t"
    "s_cbranch_scc1 L_pub12_%=\n\t"
    "s_mov_b64 exec, 1\n\t"
    "buffer_store_dword v2, off, s[28:31], s47 sc1\n\t"
    "s_mov_b64 exec, -1\n\t"
    "L_pub12_%=:\n\t"
    "ds_read_b128 v[112:115], v5 offset:32768\n\t"
    "buffer_load_dwordx4 v45, s[8:11], s42 offen offset:1024 lds\n\t"
    "v_mfma_f32_32x32x16_bf16 v[64:79], a[236:239], v[132:135], v[64:79]\n\t"
    "ds_read_b128 v[116:119], v5 offset:33792\n\t"
    "ds_read_b128 v[144:147], v44 offset:16384\n\t"
    "v_mfma_f32_32x32x16_bf16 v[20:35], a[240:243], v[136:139], v[20:35]\n\t"
    "ds_read_b128 v[120:123], v5 offset:34816\n\t"
    "s_add_u32 m0, s63, 4096\n\t"
    "s_add_u32 s42, s53, 32768\n\t"
    "buffer_load_dwordx4 v41, s[8:11], s42 offen offset:0 lds\n\t"
    "v_mfma_f32_32x32x16_bf16 v[64:79], a[244:247], v[136:139], v[64:79]\n\t"
    "ds_read_b128 v[124:127], v5 offset:35840\n\t"
    "v_mfma_f32_32x32x16_bf16 v[20:35], a[248:251], v[140:143], v[20:35]\n\t"
    "ds_read_b128 v[128:131], v5 offset:36864\n\t"
    "buffer_load_dwordx4 v45, s[8:11], s42 offen offset:1024 lds\n\t"
    "v_mfma_f32_32x32x16_bf16 v[64:79], a[252:255], v[140:143], v[64:79]\n\t"
    "ds_read_b128 v[132:135], v5 offset:37888\n\t"
    "s_waitcnt lgkmcnt(4)\n\t"
    "v_mfma_f32_32x32x16_bf16 v[20:35], v[36:39], v[144:147], v[20:35]\n\t"
    "ds_read_b128 v[136:139], v5 offset:38912\n\t"
    "v_mfma_f32_32x32x16_bf16 v[64:79], v[46:49], v[144:147], v[64:79]\n\t"
    "ds_read_b128 v[140:143], v5 offset:39936\n\t"
    "s_add_u32 m0, s63, 65536\n\t"
    "s_add_u32 s42, s53, 49152\n\t"
    "buffer_load_dwordx4 v41, s[8:11], s42 offen offset:0 lds\n\t"
    "v_mfma_f32_32x32x16_bf16 v[80:95], a[0:3], v[112:115], 0\n\t"
    "ds_read_b128 v[112:115], v5 offset:40960\n\t"
    "v_mfma_f32_32x32x16_bf16 v[80:95], a[8:11], v[116:119], v[80:95]\n\t"
    "ds_read_b128 v[116:119], v5 offset:41984\n\t"
    "buffer_load_dwordx4 v45, s[8:11], s42 offen offset:1024 lds\n\t"
    "s_waitcnt lgkmcnt(7)\n\t"
    "v_mfma_f32_32x32x16_bf16 v[80:95], a[16:19], v[120:123], v[80:95]\n\t"
    "ds_read_b128 v[120:123], v5 offset:43008\n\t"
    "v_mul_f32 v64, v20, v64\n\t"
    "v_mul_f32 v65, v21, v65\n\t"
    "v_mul_f32 v20, v146, v20\n\t"
    "v_mul_f32 v21, v146, v21\n\t"
    "v_exp_f32 v20, v20\n\t"
    "s_waitcnt lgkmcnt(7)\n\t"
    "v_mfma_f32_32x32x16_bf16 v[80:95], a[24:27], v[124:127], v[80:95]\n\t"
    "ds_read_b128 v[124:127], v5 offset:44032\n\t"
    "v_exp_f32 v21, v21\n\t"
    "v_fma_f32 v20, v147, v20, v147\n\t"
    "v_fma_f32 v21, v147, v21, v147\n\t"
    "s_waitcnt lgkmcnt(7)\n\t"
    "v_mfma_f32_32x32x16_bf16 v[80:95], a[32:35], v[128:131], v[80:95]\n\t"
    "ds_read_b128 v[128:131], v5 offset:45056\n\t"
    "s_add_u32 m0, s63, 67584\n\t"
    "s_add_u32 s42, s53, 65536\n\t"
    "buffer_load_dwordx4 v41, s[8:11], s42 offen offset:0 lds\n\t"
    "s_waitcnt lgkmcnt(7)\n\t"
    "v_mfma_f32_32x32x16_bf16 v[80:95], a[40:43], v[132:135], v[80:95]\n\t"
    "ds_read_b128 v[132:135], v5 offset:46080\n\t"
    "v_rcp_f32 v20, v20\n\t"
    "v_rcp_f32 v21, v21\n\t"
    "v_mul_f32 v64, v20, v64\n\t"
    "s_waitcnt lgkmcnt(7)\n\t"
    "v_mfma_f32_32x32x16_bf16 v[80:95], a[48:51], v[136:139], v[80:95]\n\t"
    "ds_read_b128 v[136:139], v5 offset:47104\n\t"
    "buffer_load_dwordx4 v45, s[8:11], s42 offen offset:1024 lds\n\t"
    "v_mul_f32 v65, v21, v65\n\t"
    "v_mul_f32 v66, v22, v66\n\t"
    "s_waitcnt lgkmcnt(7)\n\t"
    "v_mfma_f32_32x32x16_bf16 v[80:95], a[56:59], v[140:143], v[80:95]\n\t"
    "ds_read_b128 v[140:143], v5 offset:48128\n\t"
    "v_mul_f32 v67, v23, v67\n\t"
    "v_mul_f32 v22, v146, v22\n\t"
    "v_mul_f32 v23, v146, v23\n\t"
    "v_exp_f32 v22, v22\n\t"
    "s_waitcnt lgkmcnt(7)\n\t"
    "v_mfma_f32_32x32x16_bf16 v[80:95], a[64:67], v[112:115], v[80:95]\n\t"
    "ds_read_b128 v[112:115], v5 offset:49152\n\t"
    "v_exp_f32 v23, v23\n\t"
    "v_cvt_pk_bf16_f32 v20, v64, v65\n\t"
    "v_fma_f32 v22, v147, v22, v147\n\t"
    "v_fma_f32 v23, v147, v23, v147\n\t"
    "s_waitcnt lgkmcnt(7)\n\t"
    "v_mfma_f32_32x32x16_bf16 v[80:95], a[72:75], v[116:119], v[80:95]\n\t"
    "ds_read_b128 v[116:119], v5 offset:50176\n\t"
    "s_add_u32 m0, s63, 69632\n\t"
    "s_add_u32 s42, s53, 81920\n\t"
    "buffer_load_dwordx4 v41, s[8:11], s42 offen offset:0 lds\n\t"
    "s_waitcnt lgkmcnt(7)\n\t"
    "v_mfma_f32_32x32x16_bf16 v[80:95], a[80:83], v[120:123], v[80:95]\n\t"
    "ds_read_b128 v[120:123], v5 offset:51200\n\t"
    "v_rcp_f32 v22, v22\n\t"
    "v_rcp_f32 v23, v23\n\t"
    "v_mul_f32 v66, v22, v66\n\t"
    "s_waitcnt lgkmcnt(7)\n\t"
    "v_mfma_f32_32x32x16_bf16 v[80:95], a[88:91], v[124:127], v[80:95]\n\t"
    "ds_read_b128 v[124:127], v5 offset:52224\n\t"
    "buffer_load_dwordx4 v45, s[8:11], s42 offen offset:1024 lds\n\t"
    "v_mul_f32 v67, v23, v67\n\t"
    "v_mul_f32 v68, v24, v68\n\t"
    "s_waitcnt lgkmcnt(7)\n\t"
    "v_mfma_f32_32x32x16_bf16 v[80:95], a[96:99], v[128:131], v[80:95]\n\t"
    "ds_read_b128 v[128:131], v5 offset:53248\n\t"
    "v_mul_f32 v69, v25, v69\n\t"
    "v_mul_f32 v24, v146, v24\n\t"
    "v_mul_f32 v25, v146, v25\n\t"
    "v_exp_f32 v24, v24\n\t"
    "s_waitcnt lgkmcnt(7)\n\t"
    "v_mfma_f32_32x32x16_bf16 v[80:95], a[104:107], v[132:135], v[80:95]\n\t"
    "ds_read_b128 v[132:135], v5 offset:54272\n\t"
    "v_exp_f32 v25, v25\n\t"
    "v_cvt_pk_bf16_f32 v21, v66, v67\n\t"
    "v_fma_f32 v24, v147, v24, v147\n\t"
    "v_fma_f32 v25, v147, v25, v147\n\t"
    "s_waitcnt lgkmcnt(7)\n\t"
    "v_mfma_f32_32x32x16_bf16 v[80:95], a[112:115], v[136:139], v[80:95]\n\t"
    "ds_read_b128 v[136:139], v5 offset:55296\n\t"
    "s_add_u32 m0, s63, 71680\n\t"
    "s_add_u32 s42, s53, 98304\n\t"
    "buffer_load_dwordx4 v41, s[8:11], s42 offen offset:0 lds\n\t"
    "s_waitcnt lgkmcnt(7)\n\t"
    "v_mfma_f32_32x32x16_bf16 v[80:95], a[120:123], v[140:143], v[80:95]\n\t"
    "ds_read_b128 v[140:143], v5 offset:56320\n\t"
    "v_rcp_f32 v24, v24\n\t"
    "v_rcp_f32 v25, v25\n\t"
    "v_mul_f32 v68, v24, v68\n\t"
    "s_waitcnt lgkmcnt(7)\n\t"
    "v_mfma_f32_32x32x16_bf16 v[80:95], a[128:131], v[112:115], v[80:95]\n\t"
    "ds_read_b128 v[112:115], v5 offset:57344\n\t"
    "buffer_load_dwordx4 v45, s[8:11], s42 offen offset:1024 lds\n\t"
    "v_mul_f32 v69, v25, v69\n\t"
    "v_mul_f32 v70, v26, v70\n\t"
    "s_waitcnt lgkmcnt(7)\n\t"
    "v_mfma_f32_32x32x16_bf16 v[80:95], a[136:139], v[116:119], v[80:95]\n\t"
    "ds_read_b128 v[116:119], v5 offset:58368\n\t"
    "v_mul_f32 v71, v27, v71\n\t"
    "v_mul_f32 v26, v146, v26\n\t"
    "v_mul_f32 v27, v146, v27\n\t"
    "v_exp_f32 v26, v26\n\t"
    "s_waitcnt lgkmcnt(7)\n\t"
    "v_mfma_f32_32x32x16_bf16 v[80:95], a[144:147], v[120:123], v[80:95]\n\t"
    "ds_read_b128 v[120:123], v5 offset:59392\n\t"
    "v_exp_f32 v27, v27\n\t"
    "v_cvt_pk_bf16_f32 v22, v68, v69\n\t"
    "v_fma_f32 v26, v147, v26, v147\n\t"
    "v_fma_f32 v27, v147, v27, v147\n\t"
    "s_waitcnt lgkmcnt(7)\n\t"
    "v_mfma_f32_32x32x16_bf16 v[80:95], a[152:155], v[124:127], v[80:95]\n\t"
    "ds_read_b128 v[124:127], v5 offset:60416\n\t"
    "v_rcp_f32 v26, v26\n\t"
    "v_rcp_f32 v27, v27\n\t"
    "v_mul_f32 v70, v26, v70\n\t"
    "s_waitcnt lgkmcnt(7)\n\t"
    "v_mfma_f32_32x32x16_bf16 v[80:95], a[160:163], v[128:131], v[80:95]\n\t"
    "ds_read_b128 v[128:131], v5 offset:61440\n\t"
    "ds_read_b128 v[148:151], v44 offset:24576\n\t"
    "v_mul_f32 v71, v27, v71\n\t"
    "v_mul_f32 v72, v28, v72\n\t"
    "v_mul_f32 v73, v29, v73\n\t"
    "v_mul_f32 v28, v146, v28\n\t"
    "s_waitcnt lgkmcnt(8)\n\t"
    "v_mfma_f32_32x32x16_bf16 v[80:95], a[168:171], v[132:135], v[80:95]\n\t"
    "ds_read_b128 v[132:135], v5 offset:62464\n\t"
    "v_mul_f32 v29, v146, v29\n\t"
    "v_exp_f32 v28, v28\n\t"
    "v_exp_f32 v29, v29\n\t"
    "s_waitcnt lgkmcnt(8)\n\t"
    "v_mfma_f32_32x32x16_bf16 v[80:95], a[176:179], v[136:139], v[80:95]\n\t"
    "ds_read_b128 v[136:139], v5 offset:63488\n\t"
    "v_cvt_pk_bf16_f32 v23, v70, v71\n\t"
    "v_fma_f32 v28, v147, v28, v147\n\t"
    "v_fma_f32 v29, v147, v29, v147\n\t"
    "v_rcp_f32 v28, v28\n\t"
    "s_waitcnt lgkmcnt(8)\n\t"
    "v_mfma_f32_32x32x16_bf16 v[80:95], a[184:187], v[140:143], v[80:95]\n\t"
    "ds_read_b128 v[140:143], v5 offset:64512\n\t"
    "v_rcp_f32 v29, v29\n\t"
    "s_add_u32 s39, s45, 229376\n\t"
    "buffer_store_dwordx4 v[20:23], v19, s[16:19], s39 offen offset:0 sc1\n\t"
    "s_waitcnt lgkmcnt(8)\n\t"
    "v_mfma_f32_32x32x16_bf16 v[80:95], a[192:195], v[112:115], v[80:95]\n\t"
    "ds_read_b128 v[112:115], v5 offset:32768\n\t"
    "v_mul_f32 v72, v28, v72\n\t"
    "v_mul_f32 v73, v29, v73\n\t"
    "v_mul_f32 v74, v30, v74\n\t"
    "v_mul_f32 v75, v31, v75\n\t"
    "s_waitcnt lgkmcnt(8)\n\t"
    "v_mfma_f32_32x32x16_bf16 v[80:95], a[200:203], v[116:119], v[80:95]\n\t"
    "ds_read_b128 v[116:119], v5 offset:33792\n\t"
    "v_mul_f32 v30, v146, v30\n\t"
    "v_mul_f32 v31, v146, v31\n\t"
    "v_exp_f32 v30, v30\n\t"
    "v_exp_f32 v31, v31\n\t"
    "s_waitcnt lgkmcnt(8)\n\t"
    "v_mfma_f32_32x32x16_bf16 v[80:95], a[208:211], v[120:123], v[80:95]\n\t"
    "ds_read_b128 v[120:123], v5 offset:34816\n\t"
    "v_cvt_pk_bf16_f32 v24, v72, v73\n\t"
    "v_fma_f32 v30, v147, v30, v147\n\t"
    "v_fma_f32 v31, v147, v31, v147\n\t"
    "v_rcp_f32 v30, v30\n\t"
    "s_waitcnt lgkmcnt(8)\n\t"
    "v_mfma_f32_32x32x16_bf16 v[80:95], a[216:219], v[124:127], v[80:95]\n\t"
    "ds_read_b128 v[124:127], v5 offset:35840\n\t"
    "v_rcp_f32 v31, v31\n\t"
    "v_mul_f32 v74, v30, v74\n\t"
    "v_mul_f32 v75, v31, v75\n\t"
    "s_waitcnt lgkmcnt(8)\n\t"
    "v_mfma_f32_32x32x16_bf16 v[80:95], a[224:227], v[128:131], v[80:95]\n\t"
    "ds_read_b128 v[128:131], v5 offset:36864\n\t"
    "v_mul_f32 v76, v32, v76\n\t"
    "v_mul_f32 v77, v33, v77\n\t"
    "v_mul_f32 v32, v146, v32\n\t"
    "v_mul_f32 v33, v146, v33\n\t"
    "v_exp_f32 v32, v32\n\t"
    "s_waitcnt lgkmcnt(7)\n\t"
    "v_mfma_f32_32x32x16_bf16 v[80:95], a[232:235], v[132:135], v[80:95]\n\t"
    "ds_read_b128 v[132:135], v5 offset:37888\n\t"
    "v_exp_f32 v33, v33\n\t"
    "v_cvt_pk_bf16_f32 v25, v74, v75\n\t"
    "v_fma_f32 v32, v147, v32, v147\n\t"
    "s_waitcnt lgkmcnt(7)\n\t"
    "v_mfma_f32_32x32x16_bf16 v[80:95], a[240:243], v[136:139], v[80:95]\n\t"
    "ds_read_b128 v[136:139], v5 offset:38912\n\t"
    "v_fma_f32 v33, v147, v33, v147\n\t"
    "v_rcp_f32 v32, v32\n\t"
    "v_rcp_f32 v33, v33\n\t"
    "s_waitcnt lgkmcnt(7)\n\t"
    "v_mfma_f32_32x32x16_bf16 v[80:95], a[248:251], v[140:143], v[80:95]\n\t"
    "ds_read_b128 v[140:143], v5 offset:39936\n\t"
    "v_mul_f32 v76, v32, v76\n\t"
    "v_mul_f32 v77, v33, v77\n\t"
    "v_mul_f32 v78, v34, v78\n\t"
    "v_mul_f32 v79, v35, v79\n\t"
    "v_mul_f32 v34, v146, v34\n\t"
    "v_mfma_f32_32x32x16_bf16 v[80:95], v[36:39], v[148:151], v[80:95]\n\t"
    "buffer_load_dword v4, v3, s[28:31], s58 offen sc1\n\t"
    "v_mul_f32 v35, v146, v35\n\t"
    "v_exp_f32 v34, v34\n\t"
    "s_waitcnt lgkmcnt(7)\n\t"
    "v_mfma_f32_32x32x16_bf16 v[96:111], a[4:7], v[112:115], 0\n\t"
    "ds_read_b128 v[112:115], v5 offset:40960\n\t"
    "v_exp_f32 v35, v35\n\t"
    "v_cvt_pk_bf16_f32 v26, v76, v77\n\t"
    "v_fma_f32 v34, v147, v34, v147\n\t"
    "v_fma_f32 v35, v147, v35, v147\n\t"
    "s_waitcnt lgkmcnt(7)\n\t"
    "v_mfma_f32_32x32x16_bf16 v[96:111], a[12:15], v[116:119], v[96:111]\n\t"
    "ds_read_b128 v[116:119], v5 offset:41984\n\t"
    "v_rcp_f32 v34, v34\n\t"
    "v_rcp_f32 v35, v35\n\t"
    "v_mul_f32 v78, v34, v78\n\t"
    "s_waitcnt lgkmcnt(7)\n\t"
    "v_mfma_f32_32x32x16_bf16 v[96:111], a[20:23], v[120:123], v[96:111]\n\t"
    "ds_read_b128 v[120:123], v5 offset:43008\n\t"
    "v_mul_f32 v79, v35, v79\n\t"
    "v_cvt_pk_bf16_f32 v27, v78, v79\n\t"
    "buffer_store_dwordx4 v[24:27], v19, s[16:19], s39 offen offset:1024 sc1\n\t"
    "s_waitcnt lgkmcnt(7)\n\t"
    "v_mfma_f32_32x32x16_bf16 v[96:111], a[28:31], v[124:127], v[96:111]\n\t"
    "ds_read_b128 v[124:127], v5 offset:44032\n\t"
    "v_mul_f32 v7, v150, v80\n\t"
    "v_mul_f32 v8, v150, v81\n\t"
    "v_exp_f32 v7, v7\n\t"
    "s_waitcnt lgkmcnt(7)\n\t"
    "v_mfma_f32_32x32x16_bf16 v[96:111], a[36:39], v[128:131], v[96:111]\n\t"
    "ds_read_b128 v[128:131], v5 offset:45056\n\t"
    "v_exp_f32 v8, v8\n\t"
    "v_fma_f32 v7, v151, v7, v151\n\t"
    "v_fma_f32 v8, v151, v8, v151\n\t"
    "v_rcp_f32 v7, v7\n\t"
    "s_waitcnt lgkmcnt(7)\n\t"
    "v_mfma_f32_32x32x16_bf16 v[96:111], a[44:47], v[132:135], v[96:111]\n\t"
    "ds_read_b128 v[132:135], v5 offset:46080\n\t"
    "v_rcp_f32 v8, v8\n\t"
    "v_mul_f32 v9, v150, v82\n\t"
    "v_mul_f32 v10, v150, v83\n\t"
    "s_waitcnt lgkmcnt(7)\n\t"
    "v_mfma_f32_32x32x16_bf16 v[96:111], a[52:55], v[136:139], v[96:111]\n\t"
    "ds_read_b128 v[136:139], v5 offset:47104\n\t"
    "v_exp_f32 v9, v9\n\t"
    "v_exp_f32 v10, v10\n\t"
    "v_mul_f32 v80, v80, v7\n\t"
    "s_waitcnt lgkmcnt(7)\n\t"
    "v_mfma_f32_32x32x16_bf16 v[96:111], a[60:63], v[140:143], v[96:111]\n\t"
    "ds_read_b128 v[140:143], v5 offset:48128\n\t"
    "v_mul_f32 v81, v81, v8\n\t"
    "v_fma_f32 v9, v151, v9, v151\n\t"
    "v_fma_f32 v10, v151, v10, v151\n\t"
    "v_rcp_f32 v9, v9\n\t"
    "s_waitcnt lgkmcnt(7)\n\t"
    "v_mfma_f32_32x32x16_bf16 v[96:111], a[68:71], v[112:115], v[96:111]\n\t"
    "ds_read_b128 v[112:115], v5 offset:49152\n\t"
    "v_rcp_f32 v10, v10\n\t"
    "v_mul_f32 v7, v150, v84\n\t"
    "v_mul_f32 v8, v150, v85\n\t"
    "s_waitcnt lgkmcnt(7)\n\t"
    "v_mfma_f32_32x32x16_bf16 v[96:111], a[76:79], v[116:119], v[96:111]\n\t"
    "ds_read_b128 v[116:119], v5 offset:50176\n\t"
    "v_exp_f32 v7, v7\n\t"
    "v_exp_f32 v8, v8\n\t"
    "s_waitcnt lgkmcnt(7)\n\t"
    "v_mfma_f32_32x32x16_bf16 v[96:111], a[84:87], v[120:123], v[96:111]\n\t"
    "ds_read_b128 v[120:123], v5 offset:51200\n\t"
    "v_mul_f32 v82, v82, v9\n\t"
    "v_mul_f32 v83, v83, v10\n\t"
    "v_fma_f32 v7, v151, v7, v151\n\t"
    "v_fma_f32 v8, v151, v8, v151\n\t"
    "v_rcp_f32 v7, v7\n\t"
    "s_waitcnt lgkmcnt(7)\n\t"
    "v_mfma_f32_32x32x16_bf16 v[96:111], a[92:95], v[124:127], v[96:111]\n\t"
    "ds_read_b128 v[124:127], v5 offset:52224\n\t"
    "s_waitcnt vmcnt(1)\n\t"
    "v_cmp_ne_u32 vcc, v4, v2\n\t"
    "s_mov_b32 s41, 40000\n\t"
    "s_nop 3\n\t"
    "s_cbranch_vccz L_qe8_%=\n\t"
    "L_ql9_%=:\n\t"
    "buffer_load_dword v4, v3, s[28:31], s58 offen sc1\n\t"
    "s_sub_u32 s41, s41, 1\n\t"
    "s_cbranch_scc1 L_qe8_%=\n\t"
    "s_waitcnt vmcnt(0)\n\t"
    "v_cmp_ne_u32 vcc, v4, v2\n\t"
    "s_nop 4\n\t"
    "s_cbranch_vccnz L_ql9_%=\n\t"
    "L_qe8_%=:\n\t"
    "buffer_load_dwordx4 a[0:3], v19, s[28:31], s54 offen offset:0 sc1\n\t"
    "s_waitcnt lgkmcnt(7)\n\t"
    "v_mfma_f32_32x32x16_bf16 v[96:111], a[100:103], v[128:131], v[96:111]\n\t"
    "ds_read_b128 v[128:131], v5 offset:53248\n\t"
    "v_rcp_f32 v8, v8\n\t"
    "v_mul_f32 v9, v150, v86\n\t"
    "v_mul_f32 v10, v150, v87\n\t"
    "s_waitcnt lgkmcnt(7)\n\t"
    "v_mfma_f32_32x32x16_bf16 v[96:111], a[108:111], v[132:135], v[96:111]\n\t"
    "ds_read_b128 v[132:135], v5 offset:54272\n\t"
    "buffer_load_dwordx4 a[8:11], v19, s[28:31], s54 offen offset:1024 sc1\n\t"
    "v_exp_f32 v9, v9\n\t"
    "s_waitcnt lgkmcnt(7)\n\t"
    "v_mfma_f32_32x32x16_bf16 v[96:111], a[116:119], v[136:139], v[96:111]\n\t"
    "ds_read_b128 v[136:139], v5 offset:55296\n\t"
    "v_exp_f32 v10, v10\n\t"
    "v_mul_f32 v84, v84, v7\n\t"
    "v_mul_f32 v85, v85, v8\n\t"
    "s_waitcnt lgkmcnt(7)\n\t"
    "v_mfma_f32_32x32x16_bf16 v[96:111], a[124:127], v[140:143], v[96:111]\n\t"
    "ds_read_b128 v[140:143], v5 offset:56320\n\t"
    "buffer_load_dwordx4 a[16:19], v19, s[28:31], s54 offen offset:2048 sc1\n\t"
    "v_fma_f32 v9, v151, v9, v151\n\t"
    "v_fma_f32 v10, v151, v10, v151\n\t"
    "s_waitcnt lgkmcnt(7)\n\t"
    "v_mfma_f32_32x32x16_bf16 v[96:111], a[132:135], v[112:115], v[96:111]\n\t"
    "ds_read_b128 v[112:115], v5 offset:57344\n\t"
    "v_rcp_f32 v9, v9\n\t"
    "v_rcp_f32 v10, v10\n\t"
    "s_waitcnt lgkmcnt(7)\n\t"
    "v_mfma_f32_32x32x16_bf16 v[96:111], a[140:143], v[116:119], v[96:111]\n\t"
    "ds_read_b128 v[116:119], v5 offset:58368\n\t"
    "buffer_load_dwordx4 a[24:27], v19, s[28:31], s54 offen offset:3072 sc1\n\t"
    "v_mul_f32 v7, v150, v88\n\t"
    "v_mul_f32 v8, v150, v89\n\t"
    "s_waitcnt lgkmcnt(7)\n\t"
    "v_mfma_f32_32x32x16_bf16 v[96:111], a[148:151], v[120:123], v[96:111]\n\t"
    "ds_read_b128 v[120:123], v5 offset:59392\n\t"
    "v_exp_f32 v7, v7\n\t"
    "v_exp_f32 v8, v8\n\t"
    "s_waitcnt lgkmcnt(7)\n\t"
    "v_mfma_f32_32x32x16_bf16 v[96:111], a[156:159], v[124:127], v[96:111]\n\t"
    "ds_read_b128 v[124:127], v5 offset:60416\n\t"
    "buffer_load_dwordx4 a[32:35], v42, s[28:31], s54 offen offset:0 sc1\n\t"
    "v_mul_f32 v86, v86, v9\n\t"
    "v_mul_f32 v87, v87, v10\n\t"
    "s_waitcnt lgkmcnt(7)\n\t"
    "v_mfma_f32_32x32x16_bf16 v[96:111], a[164:167], v[128:131], v[96:111]\n\t"
    "ds_read_b128 v[128:131], v5 offset:61440\n\t"
    "v_fma_f32 v7, v151, v7, v151\n\t"
    "v_fma_f32 v8, v151, v8, v151\n\t"
    "v_rcp_f32 v7, v7\n\t"
    "s_waitcnt lgkmcnt(7)\n\t"
    "v_mfma_f32_32x32x16_bf16 v[96:111], a[172:175], v[132:135], v[96:111]\n\t"
    "ds_read_b128 v[132:135], v5 offset:62464\n\t"
    "buffer_load_dwordx4 a[40:43], v42, s[28:31], s54 offen offset:1024 sc1\n\t"
    "v_rcp_f32 v8, v8\n\t"
    "s_waitcnt lgkmcnt(7)\n\t"
    "v_mfma_f32_32x32x16_bf16 v[96:111], a[180:183], v[136:139], v[96:111]\n\t"
    "ds_read_b128 v[136:139], v5 offset:63488\n\t"
    "v_mul_f32 v9, v150, v90\n\t"
    "v_mul_f32 v10, v150, v91\n\t"
    "v_exp_f32 v9, v9\n\t"
    "s_waitcnt lgkmcnt(7)\n\t"
    "v_mfma_f32_32x32x16_bf16 v[96:111], a[188:191], v[140:143], v[96:111]\n\t"
    "ds_read_b128 v[140:143], v5 offset:64512\n\t"
    "buffer_load_dwordx4 a[48:51], v42, s[28:31], s54 offen offset:2048 sc1\n\t"
    "v_exp_f32 v10, v10\n\t"
    "v_mul_f32 v88, v88, v7\n\t"
    "s_waitcnt lgkmcnt(7)\n\t"
    "v_mfma_f32_32x32x16_bf16 v[96:111], a[196:199], v[112:115], v[96:111]\n\t"
    "v_mul_f32 v89, v89, v8\n\t"
    "v_fma_f32 v9, v151, v9, v151\n\t"
    "v_fma_f32 v10, v151, v10, v151\n\t"
    "v_rcp_f32 v9, v9\n\t"
    "s_waitcnt lgkmcnt(6)\n\t"
    "v_mfma_f32_32x32x16_bf16 v[96:111], a[204:207], v[116:119], v[96:111]\n\t"
    "buffer_load_dwordx4 a[56:59], v19, s[28:31], s55 offen offset:0 sc1\n\t"
    "v_rcp_f32 v10, v10\n\t"
    "v_mul_f32 v7, v150, v92\n\t"
    "s_waitcnt lgkmcnt(5)\n\t"
    "v_mfma_f32_32x32x16_bf16 v[96:111], a[212:215], v[120:123], v[96:111]\n\t"
    "v_mul_f32 v8, v150, v93\n\t"
    "v_exp_f32 v7, v7\n\t"
    "v_exp_f32 v8, v8\n\t"
    "s_waitcnt lgkmcnt(4)\n\t"
    "v_mfma_f32_32x32x16_bf16 v[96:111], a[220:223], v[124:127], v[96:111]\n\t"
    "buffer_load_dwordx4 a[64:67], v19, s[28:31], s55 offen offset:1024 sc1\n\t"
    "v_mul_f32 v90, v90, v9\n\t"
    "v_mul_f32 v91, v91, v10\n\t"
    "v_fma_f32 v7, v151, v7, v151\n\t"
    "s_waitcnt lgkmcnt(3)\n\t"
    "v_mfma_f32_32x32x16_bf16 v[96:111], a[228:231], v[128:131], v[96:111]\n\t"
    "v_fma_f32 v8, v151, v8, v151\n\t"
    "v_rcp_f32 v7, v7\n\t"
    "v_rcp_f32 v8, v8\n\t"
    "v_mul_f32 v9, v150, v94\n\t"
    "s_waitcnt lgkmcnt(2)\n\t"
    "v_mfma_f32_32x32x16_bf16 v[96:111], a[236:239], v[132:135], v[96:111]\n\t"
    "buffer_load_dwordx4 a[72:75], v19, s[28:31], s55 offen offset:2048 sc1\n\t"
    "v_mul_f32 v10, v150, v95\n\t"
    "v_exp_f32 v9, v9\n\t"
    "s_waitcnt lgkmcnt(1)\n\t"
    "v_mfma_f32_32x32x16_bf16 v[96:111], a[244:247], v[136:139], v[96:111]\n\t"
    "v_exp_f32 v10, v10\n\t"
    "v_mul_f32 v92, v92, v7\n\t"
    "v_mul_f32 v93, v93, v8\n\t"
    "v_fma_f32 v9, v151, v9, v151\n\t"
    "s_waitcnt lgkmcnt(0)\n\t"
    "v_mfma_f32_32x32x16_bf16 v[96:111], a[252:255], v[140:143], v[96:111]\n\t"
    "buffer_load_dwordx4 a[80:83], v19, s[28:31], s55 offen offset:3072 sc1\n\t"
    "v_fma_f32 v10, v151, v10, v151\n\t"
    "v_rcp_f32 v9, v9\n\t"
    "v_mfma_f32_32x32x16_bf16 v[96:111], v[46:49], v[148:151], v[96:111]\n\t"
    "v_rcp_f32 v10, v10\n\t"
    "v_mul_f32 v94, v94, v9\n\t"
    "v_mul_f32 v95, v95, v10\n\t"
    "buffer_load_dwordx4 a[88:91], v42, s[28:31], s55 offen offset:0 sc1\n\t"
    "s_add_u32 s39, s45, 344064\n\t"
    "s_nop 6\n\t"
    "v_pk_mul_f32 v[96:97], v[80:81], v[96:97]\n\t"
    "v_pk_mul_f32 v[98:99], v[82:83], v[98:99]\n\t"
    "v_pk_mul_f32 v[100:101], v[84:85], v[100:101]\n\t"
    "v_pk_mul_f32 v[102:103], v[86:87], v[102:103]\n\t"
    "v_cvt_pk_bf16_f32 v80, v96, v97\n\t"
    "v_cvt_pk_bf16_f32 v81, v98, v99\n\t"
    "v_cvt_pk_bf16_f32 v82, v100, v101\n\t"
    "v_cvt_pk_bf16_f32 v83, v102, v103\n\t"
    "buffer_store_dwordx4 v[80:83], v19, s[16:19], s39 offen offset:0 sc1\n\t"
    "v_pk_mul_f32 v[104:105], v[88:89], v[104:105]\n\t"
    "v_pk_mul_f32 v[106:107], v[90:91], v[106:107]\n\t"
    "v_pk_mul_f32 v[108:109], v[92:93], v[108:109]\n\t"
    "v_pk_mul_f32 v[110:111], v[94:95], v[110:111]\n\t"
    "v_cvt_pk_bf16_f32 v88, v104, v105\n\t"
    "v_cvt_pk_bf16_f32 v89, v106, v107\n\t"
    "v_cvt_pk_bf16_f32 v90, v108, v109\n\t"
    "v_cvt_pk_bf16_f32 v91, v110, v111\n\t"
    "buffer_store_dwordx4 v[88:91], v19, s[16:19], s39 offen offset:1024 sc1\n\t"
    "s_add_u32 s47, s47, 65536\n\t"
    "buffer_load_dwordx4 a[96:99], v42, s[28:31], s55 offen offset:1024 sc1\n\t"
    "buffer_load_dwordx4 a[104:107], v42, s[28:31], s55 offen offset:2048 sc1\n\t"
    "buffer_load_dwordx4 a[112:115], v19, s[28:31], s56 offen offset:0 sc1\n\t"
    "buffer_load_dwordx4 a[120:123], v19, s[28:31], s56 offen offset:1024 sc1\n\t"
    "s_lshl2_add_u32 s39, s65, s33\n\t"
    "s_lshl_b32 s39, s39, 10\n\t"
    "s_lshl_b32 s40, s49, 15\n\t"
    "s_add_u32 s39, s39, s40\n\t"
    "s_add_u32 s66, s39, 3145728\n\t"
    "s_add_u32 s67, s39, 3211264\n\t"
    "s_add_u32 s58, s58, 65536\n\t"
    "v_add_u32 v5, s63, v5\n\t"
    "v_xor_b32 v44, 8192, v5\n\t"
    "v_xor_b32 v62, 16384, v5\n\t"
    "v_xor_b32 v63, 24576, v5\n\t"
    "v_lshlrev_b32 v144, 10, v17\n\t"
    "v_lshl_add_u32 v144, v18, 4, v144\n\t"
    "s_lshl_b32 s39, s49, 15\n\t"
    "s_lshl_b32 s40, s65, 7\n\t"
    "s_add_u32 s39, s39, s40\n\t"
    "s_lshl_b32 s40, s33, 5\n\t"
    "s_add_u32 s49, s39, s40\n\t"
    "s_add_u32 s44, s49, 65536\n\t"
    "s_waitcnt vmcnt(17)\n\t"
    "v_mfma_f32_32x32x16_bf16 v[20:35], v[50:53], a[0:3], 0\n\t"
    "buffer_load_dwordx4 a[128:131], v19, s[28:31], s56 offen offset:2048 sc1\n\t"
    "v_mfma_f32_32x32x16_bf16 v[64:79], v[54:57], a[0:3], 0\n\t"
    "s_waitcnt vmcnt(17)\n\t"
    "v_mfma_f32_32x32x16_bf16 v[20:35], v[58:61], a[8:11], v[20:35]\n\t"
    "buffer_load_dwordx4 a[136:139], v19, s[28:31], s56 offen offset:3072 sc1\n\t"
    "v_mfma_f32_32x32x16_bf16 v[64:79], v[152:155], a[8:11], v[64:79]\n\t"
    "s_waitcnt vmcnt(17)\n\t"
    "v_mfma_f32_32x32x16_bf16 v[20:35], v[156:159], a[16:19], v[20:35]\n\t"
    "buffer_load_dwordx4 a[144:147], v42, s[28:31], s56 offen offset:0 sc1\n\t"
    "v_mfma_f32_32x32x16_bf16 v[64:79], v[160:163], a[16:19], v[64:79]\n\t"
    "s_waitcnt vmcnt(17)\n\t"
    "v_mfma_f32_32x32x16_bf16 v[20:35], v[164:167], a[24:27], v[20:35]\n\t"
    "buffer_load_dwordx4 a[152:155], v42, s[28:31], s56 offen offset:1024 sc1\n\t"
    "v_mfma_f32_32x32x16_bf16 v[64:79], v[168:171], a[24:27], v[64:79]\n\t"
    "s_waitcnt vmcnt(17)\n\t"
    "v_mfma_f32_32x32x16_bf16 v[20:35], v[172:175], a[32:35], v[20:35]\n\t"
    "buffer_load_dwordx4 a[160:163], v42, s[28:31], s56 offen offset:2048 sc1\n\t"
    "v_mfma_f32_32x32x16_bf16 v[64:79], v[176:179], a[32:35], v[64:79]\n\t"
    "s_waitcnt vmcnt(17)\n\t"
    "v_mfma_f32_32x32x16_bf16 v[20:35], v[180:183], a[40:43], v[20:35]\n\t"
    "buffer_load_dwordx4 a[168:171], v19, s[28:31], s57 offen offset:0 sc1\n\t"
    "v_mfma_f32_32x32x16_bf16 v[64:79], v[184:187], a[40:43], v[64:79]\n\t"
    "s_waitcnt vmcnt(17)\n\t"
    "v_mfma_f32_32x32x16_bf16 v[20:35], v[188:191], a[48:51], v[20:35]\n\t"
    "buffer_load_dwordx4 a[176:179], v19, s[28:31], s57 offen offset:1024 sc1\n\t"
    "v_mfma_f32_32x32x16_bf16 v[64:79], v[192:195], a[48:51], v[64:79]\n\t"
    "s_waitcnt vmcnt(17)\n\t"
    "v_mfma_f32_32x32x16_bf16 v[20:35], v[196:199], a[56:59], v[20:35]\n\t"
    "s_waitcnt vmcnt(11)\n\t"
    "s_barrier\n\t"
    "s_cmp_lg_u32 s33, 1\n\t"
    "s_cbranch_scc1 L_pub13_%=\n\t"
    "s_mov_b64 exec, 1\n\t"
    "buffer_store_dword v2, off, s[28:31], s47 sc1\n\t"
    "s_mov_b64 exec, -1\n\t"
    "L_pub13_%=:\n\t"
    "buffer_load_dwordx4 a[184:187], v19, s[28:31], s57 offen offset:2048 sc1\n\t"
    "v_mfma_f32_32x32x16_bf16 v[64:79], v[200:203], a[56:59], v[64:79]\n\t"
    "v_mfma_f32_32x32x16_bf16 v[20:35], v[204:207], a[64:67], v[20:35]\n\t"
    "buffer_load_dwordx4 a[192:195], v19, s[28:31], s57 offen offset:3072 sc1\n\t"
    "v_mfma_f32_32x32x16_bf16 v[64:79], v[208:211], a[64:67], v[64:79]\n\t"
    "v_mfma_f32_32x32x16_bf16 v[20:35], v[212:215], a[72:75], v[20:35]\n\t"
    "buffer_load_dwordx4 a[200:203], v42, s[28:31], s57 offen offset:0 sc1\n\t"
    "v_mfma_f32_32x32x16_bf16 v[64:79], v[216:219], a[72:75], v[64:79]\n\t"
    "v_mfma_f32_32x32x16_bf16 v[20:35], v[220:223], a[80:83], v[20:35]\n\t"
    "ds_read_b64_tr_b16 v[112:113], v6 offset:6144\n\t"
    "ds_read_b64_tr_b16 v[114:115], v6 offset:7168\n\t"
    "buffer_load_dwordx4 a[208:211], v42, s[28:31], s57 offen offset:1024 sc1\n\t"
    "v_mfma_f32_32x32x16_bf16 v[64:79], v[224:227], a[80:83], v[64:79]\n\t"
    "ds_read_b64_tr_b16 v[116:117], v13 offset:6144\n\t"
    "ds_read_b64_tr_b16 v[118:119], v13 offset:7168\n\t"
    "v_mfma_f32_32x32x16_bf16 v[20:35], v[228:231], a[88:91], v[20:35]\n\t"
    "ds_read_b64_tr_b16 v[120:121], v6 offset:32768\n\t"
    "ds_read_b64_tr_b16 v[122:123], v6 offset:33792\n\t"
    "buffer_load_dwordx4 a[216:219], v42, s[28:31], s57 offen offset:2048 sc1\n\t"
    "v_mfma_f32_32x32x16_bf16 v[64:79], v[232:235], a[88:91], v[64:79]\n\t"
    "ds_read_b64_tr_b16 v[124:125], v13 offset:32768\n\t"
    "ds_read_b64_tr_b16 v[126:127], v13 offset:33792\n\t"
    "s_waitcnt vmcnt(15)\n\t"
    "v_mfma_f32_32x32x16_bf16 v[20:35], v[236:239], a[96:99], v[20:35]\n\t"
    "ds_read_b64_tr_b16 v[128:129], v6 offset:34816\n\t"
    "ds_read_b64_tr_b16 v[130:131], v6 offset:35840\n\t"
    "buffer_load_dwordx4 v[36:39], v19, s[28:31], s66 offen sc1\n\t"
    "v_mfma_f32_32x32x16_bf16 v[64:79], v[240:243], a[96:99], v[64:79]\n\t"
    "ds_read_b64_tr_b16 v[132:133], v13 offset:34816\n\t"
    "ds_read_b64_tr_b16 v[134:135], v13 offset:35840\n\t"
    "s_waitcnt vmcnt(15) lgkmcnt(10)\n\t"
    "v_mfma_f32_32x32x16_bf16 v[20:35], v[112:115], a[104:107], v[20:35]\n\t"
    "ds_read_b64_tr_b16 v[136:137], v6 offset:36864\n\t"
    "ds_read_b64_tr_b16 v[138:139], v6 offset:37888\n\t"
    "buffer_load_dwordx4 v[46:49], v19, s[28:31], s67 offen sc1\n\t"
    "s_waitcnt lgkmcnt(10)\n\t"
    "v_mfma_f32_32x32x16_bf16 v[64:79], v[116:119], a[104:107], v[64:79]\n\t"
    "ds_read_b64_tr_b16 v[140:141], v13 offset:36864\n\t"
    "ds_read_b64_tr_b16 v[142:143], v13 offset:37888\n\t"
    "s_waitcnt vmcnt(15) lgkmcnt(10)\n\t"
    "v_mfma_f32_32x32x16_bf16 v[20:35], v[120:123], a[112:115], v[20:35]\n\t"
    "ds_read_b64_tr_b16 v[112:113], v6 offset:38912\n\t"
    "ds_read_b64_tr_b16 v[114:115], v6 offset:39936\n\t"
    "s_waitcnt lgkmcnt(10)\n\t"
    "v_mfma_f32_32x32x16_bf16 v[64:79], v[124:127], a[112:115], v[64:79]\n\t"
    "ds_read_b64_tr_b16 v[116:117], v13 offset:38912\n\t"
    "ds_read_b64_tr_b16 v[118:119], v13 offset:39936\n\t"
    "s_waitcnt vmcnt(14) lgkmcnt(10)\n\t"
    "v_mfma_f32_32x32x16_bf16 v[20:35], v[128:131], a[120:123], v[20:35]\n\t"
    "ds_read_b64_tr_b16 v[120:121], v40 offset:0\n\t"
    "ds_read_b64_tr_b16 v[122:123], v40 offset:1024\n\t"
    "s_waitcnt lgkmcnt(10)\n\t"
    "v_mfma_f32_32x32x16_bf16 v[64:79], v[132:135], a[120:123], v[64:79]\n\t"
    "ds_read_b64_tr_b16 v[124:125], v43 offset:0\n\t"
    "ds_read_b64_tr_b16 v[126:127], v43 offset:1024\n\t"
    "s_waitcnt vmcnt(13) lgkmcnt(10)\n\t"
    "v_mfma_f32_32x32x16_bf16 v[20:35], v[136:139], a[128:131], v[20:35]\n\t"
    "ds_read_b64_tr_b16 v[128:129], v40 offset:2048\n\t"
    "ds_read_b64_tr_b16 v[130:131], v40 offset:3072\n\t"
    "s_waitcnt lgkmcnt(10)\n\t"
    "v_mfma_f32_32x32x16_bf16 v[64:79], v[140:143], a[128:131], v[64:79]\n\t"
    "ds_read_b64_tr_b16 v[132:133], v43 offset:2048\n\t"
    "ds_read_b64_tr_b16 v[134:135], v43 offset:3072\n\t"
    "s_waitcnt vmcnt(12) lgkmcnt(10)\n\t"
    "v_mfma_f32_32x32x16_bf16 v[20:35], v[112:115], a[136:139], v[20:35]\n\t"
    "ds_read_b64_tr_b16 v[136:137], v40 offset:4096\n\t"
    "ds_read_b64_tr_b16 v[138:139], v40 offset:5120\n\t"
    "s_waitcnt lgkmcnt(10)\n\t"
    "v_mfma_f32_32x32x16_bf16 v[64:79], v[116:119], a[136:139], v[64:79]\n\t"
    "ds_read_b64_tr_b16 v[140:141], v43 offset:4096\n\t"
    "ds_read_b64_tr_b16 v[142:143], v43 offset:5120\n\t"
    "s_waitcnt vmcnt(11) lgkmcnt(10)\n\t"
    "v_mfma_f32_32x32x16_bf16 v[20:35], v[120:123], a[144:147], v[20:35]\n\t"
    "ds_read_b64_tr_b16 v[112:113], v6 offset:0\n\t"
    "ds_read_b64_tr_b16 v[114:115], v6 offset:1024\n\t"
    "s_waitcnt lgkmcnt(10)\n\t"
    "v_mfma_f32_32x32x16_bf16 v[64:79], v[124:127], a[144:147], v[64:79]\n\t"
    "ds_read_b64_tr_b16 v[116:117], v13 offset:0\n\t"
    "ds_read_b64_tr_b16 v[118:119], v13 offset:1024\n\t"
    "s_waitcnt vmcnt(10) lgkmcnt(10)\n\t"
    "v_mfma_f32_32x32x16_bf16 v[20:35], v[128:131], a[152:155], v[20:35]\n\t"
    "ds_read_b64_tr_b16 v[120:121], v6 offset:2048\n\t"
    "ds_read_b64_tr_b16 v[122:123], v6 offset:3072\n\t"
    "s_waitcnt lgkmcnt(10)\n\t"
    "v_mfma_f32_32x32x16_bf16 v[64:79], v[132:135], a[152:155], v[64:79]\n\t"
    "ds_read_b64_tr_b16 v[124:125], v13 offset:2048\n\t"
    "ds_read_b64_tr_b16 v[126:127], v13 offset:3072\n\t"
    "s_waitcnt vmcnt(9) lgkmcnt(10)\n\t"
    "v_mfma_f32_32x32x16_bf16 v[20:35], v[136:139], a[160:163], v[20:35]\n\t"
    "ds_read_b64_tr_b16 v[128:129], v6 offset:4096\n\t"
    "ds_read_b64_tr_b16 v[130:131], v6 offset:5120\n\t"
    "buffer_load_dword v4, v3, s[28:31], s58 offen sc1\n\t"
    "s_waitcnt lgkmcnt(10)\n\t"
    "v_mfma_f32_32x32x16_bf16 v[64:79], v[140:143], a[160:163], v[64:79]\n\t"
    "ds_read_b64_tr_b16 v[132:133], v13 offset:4096\n\t"
    "ds_read_b64_tr_b16 v[134:135], v13 offset:5120\n\t"
    "s_waitcnt vmcnt(9) lgkmcnt(10)\n\t"
    "v_mfma_f32_32x32x16_bf16 v[20:35], v[112:115], a[168:171], v[20:35]\n\t"
    "ds_read_b64_tr_b16 v[136:137], v14 offset:0\n\t"
    "ds_read_b64_tr_b16 v[138:139], v14 offset:1024\n\t"
    "s_waitcnt lgkmcnt(10)\n\t"
    "v_mfma_f32_32x32x16_bf16 v[64:79], v[116:119], a[168:171], v[64:79]\n\t"
    "ds_read_b64_tr_b16 v[140:141], v16 offset:0\n\t"
    "ds_read_b64_tr_b16 v[142:143], v16 offset:1024\n\t"
    "s_waitcnt vmcnt(8) lgkmcnt(10)\n\t"
    "v_mfma_f32_32x32x16_bf16 v[20:35], v[120:123], a[176:179], v[20:35]\n\t"
    "ds_read_b64_tr_b16 v[112:113], v14 offset:2048\n\t"
    "ds_read_b64_tr_b16 v[114:115], v14 offset:3072\n\t"
    "s_waitcnt lgkmcnt(10)\n\t"
    "v_mfma_f32_32x32x16_bf16 v[64:79], v[124:127], a[176:179], v[64:79]\n\t"
    "ds_read_b64_tr_b16 v[116:117], v16 offset:2048\n\t"
    "ds_read_b64_tr_b16 v[118:119], v16 offset:3072\n\t"
    "s_waitcnt vmcnt(7) lgkmcnt(10)\n\t"
    "v_mfma_f32_32x32x16_bf16 v[20:35], v[128:131], a[184:187], v[20:35]\n\t"
    "ds_read_b64_tr_b16 v[120:121], v14 offset:4096\n\t"
    "ds_read_b64_tr_b16 v[122:123], v14 offset:5120\n\t"
    "s_waitcnt lgkmcnt(10)\n\t"
    "v_mfma_f32_32x32x16_bf16 v[64:79], v[132:135], a[184:187], v[64:79]\n\t"
    "ds_read_b64_tr_b16 v[124:125], v16 offset:4096\n\t"
    "ds_read_b64_tr_b16 v[126:127], v16 offset:5120\n\t"
    "s_waitcnt vmcnt(6) lgkmcnt(10)\n\t"
    "v_mfma_f32_32x32x16_bf16 v[20:35], v[136:139], a[192:195], v[20:35]\n\t"
    "ds_read_b64_tr_b16 v[128:129], v14 offset:6144\n\t"
    "ds_read_b64_tr_b16 v[130:131], v14 offset:7168\n\t"
    "s_waitcnt lgkmcnt(10)\n\t"
    "v_mfma_f32_32x32x16_bf16 v[64:79], v[140:143], a[192:195], v[64:79]\n\t"
    "ds_read_b64_tr_b16 v[132:133], v16 offset:6144\n\t"
    "ds_read_b64_tr_b16 v[134:135], v16 offset:7168\n\t"
    "s_waitcnt vmcnt(5) lgkmcnt(10)\n\t"
    "v_mfma_f32_32x32x16_bf16 v[20:35], v[112:115], a[200:203], v[20:35]\n\t"
    "s_waitcnt lgkmcnt(8)\n\t"
    "v_mfma_f32_32x32x16_bf16 v[64:79], v[116:119], a[200:203], v[64:79]\n\t"
    "s_waitcnt vmcnt(4) lgkmcnt(6)\n\t"
    "v_mfma_f32_32x32x16_bf16 v[20:35], v[120:123], a[208:211], v[20:35]\n\t"
    "s_waitcnt lgkmcnt(4)\n\t"
    "v_mfma_f32_32x32x16_bf16 v[64:79], v[124:127], a[208:211], v[64:79]\n\t"
    "s_waitcnt vmcnt(3) lgkmcnt(0)\n\t"
    "v_mfma_f32_32x32x16_bf16 v[64:79], v[132:135], a[216:219], v[64:79]\n\t"
    "v_mfma_f32_32x32x16_bf16 v[20:35], v[128:131], a[216:219], v[20:35]\n\t"
    "s_waitcnt vmcnt(0)\n\t"
    "v_cmp_ne_u32 vcc, v4, v2\n\t"
    "s_mov_b32 s41, 40000\n\t"
    "s_nop 3\n\t"
    "s_cbranch_vccz L_qe14_%=\n\t"
    "L_ql15_%=:\n\t"
    "buffer_load_dword v4, v3, s[28:31], s58 offen sc1\n\t"
    "s_sub_u32 s41, s41, 1\n\t"
    "s_cbranch_scc1 L_qe14_%=\n\t"
    "s_waitcnt vmcnt(0)\n\t"
    "v_cmp_ne_u32 vcc, v4, v2\n\t"
    "s_nop 4\n\t"
    "s_cbranch_vccnz L_ql15_%=\n\t"
    "L_qe14_%=:\n\t"
    "s_nop 2\n\t"
    "ds_write_b128 v5, v[72:75] offset:36864\n\t"
    "buffer_load_dwordx4 a[4:7], v19, s[28:31], s59 offen offset:0 sc1\n\t"
    "ds_write_b128 v5, v[76:79] offset:37888\n\t"
    "ds_write_b128 v5, v[64:67] offset:34816\n\t"
    "ds_write_b128 v5, v[68:71] offset:35840\n\t"
    "ds_write_b128 v5, v[28:31] offset:32768\n\t"
    "buffer_load_dwordx4 a[12:15], v19, s[28:31], s59 offen offset:1024 sc1\n\t"
    "ds_write_b128 v5, v[32:35] offset:33792\n\t"
    "v_lshlrev_b32 v7, 16, v36\n\t"
    "v_and_b32 v8, 0xffff0000, v36\n\t"
    "v_add_f32 v20, v20, v7\n\t"
    "buffer_load_dwordx4 a[20:23], v19, s[28:31], s59 offen offset:2048 sc1\n\t"
    "v_add_f32 v21, v21, v8\n\t"
    "v_lshlrev_b32 v7, 16, v37\n\t"
    "v_and_b32 v8, 0xffff0000, v37\n\t"
    "v_add_f32 v22, v22, v7\n\t"
    "buffer_load_dwordx4 a[196:199], v19, s[28:31], s62 offen offset:3072 sc1\n\t"
    "v_add_f32 v23, v23, v8\n\t"
    "v_lshlrev_b32 v7, 16, v38\n\t"
    "v_and_b32 v8, 0xffff0000, v38\n\t"
    "v_add_f32 v24, v24, v7\n\t"
    "buffer_load_dwordx4 a[204:207], v42, s[28:31], s62 offen offset:0 sc1\n\t"
    "v_add_f32 v25, v25, v8\n\t"
    "v_lshlrev_b32 v7, 16, v39\n\t"
    "v_and_b32 v8, 0xffff0000, v39\n\t"
    "v_add_f32 v26, v26, v7\n\t"
    "buffer_load_dwordx4 a[108:111], v42, s[28:31], s60 offen offset:2048 sc1\n\t"
    "v_add_f32 v27, v27, v8\n\t"
    "s_waitcnt lgkmcnt(0)\n\t"
    "s_barrier\n\t"
    "ds_read_b128 v[64:67], v44 offset:32768\n\t"
    "buffer_load_dwordx4 a[116:119], v19, s[28:31], s61 offen offset:0 sc1\n\t"
    "ds_read_b128 v[68:71], v62 offset:34816\n\t"
    "ds_read_b128 v[72:75], v63 offset:36864\n\t"
    "ds_read_b128 v[76:79], v44 offset:33792\n\t"
    "ds_read_b128 v[28:31], v62 offset:35840\n\t"
    "buffer_load_dwordx4 a[124:127], v19, s[28:31], s61 offen offset:1024 sc1\n\t"
    "ds_read_b128 v[32:35], v63 offset:37888\n\t"
    "s_waitcnt lgkmcnt(4)\n\t"
    "v_pk_add_f32 v[64:65], v[64:65], v[68:69]\n\t"
    "s_waitcnt lgkmcnt(3)\n\t"
    "v_pk_add_f32 v[20:21], v[20:21], v[72:73]\n\t"
    "v_pk_add_f32 v[66:67], v[66:67], v[70:71]\n\t"
    "buffer_load_dwordx4 a[132:135], v19, s[28:31], s61 offen offset:2048 sc1\n\t"
    "v_pk_add_f32 v[22:23], v[22:23], v[74:75]\n\t"
    "v_pk_add_f32 v[20:21], v[20:21], v[64:65]\n\t"
    "v_pk_add_f32 v[22:23], v[22:23], v[66:67]\n\t"
    "v_cvt_pk_bf16_f32 v36, v20, v21\n\t"
    "buffer_load_dwordx4 a[140:143], v19, s[28:31], s61 offen offset:3072 sc1\n\t"
    "v_cvt_pk_bf16_f32 v37, v22, v23\n\t"
    "s_waitcnt lgkmcnt(1)\n\t"
    "v_pk_add_f32 v[76:77], v[76:77], v[28:29]\n\t"
    "s_waitcnt lgkmcnt(0)\n\t"
    "v_pk_add_f32 v[24:25], v[24:25], v[32:33]\n\t"
    "v_pk_add_f32 v[78:79], v[78:79], v[30:31]\n\t"
    "buffer_load_dwordx4 a[148:151], v42, s[28:31], s61 offen offset:0 sc1\n\t"
    "v_pk_add_f32 v[26:27], v[26:27], v[34:35]\n\t"
    "v_pk_add_f32 v[24:25], v[24:25], v[76:77]\n\t"
    "v_pk_add_f32 v[26:27], v[26:27], v[78:79]\n\t"
    "v_cvt_pk_bf16_f32 v38, v24, v25\n\t"
    "buffer_load_dwordx4 a[156:159], v42, s[28:31], s61 offen offset:1024 sc1\n\t"
    "v_cvt_pk_bf16_f32 v39, v26, v27\n\t"
    "v_permlane32_swap_b32 v36, v38\n\t"
    "s_nop 0\n\t"
    "v_permlane32_swap_b32 v37, v39\n\t"
    "s_waitcnt vmcnt(11)\n\t"
    "v_mfma_f32_32x32x16_bf16 v[80:95], v[50:53], a[4:7], 0\n\t"
    "ds_read_b64_tr_b16 v[136:137], v14 offset:0\n\t"
    "ds_read_b64_tr_b16 v[138:139], v14 offset:1024\n\t"
    "buffer_load_dwordx4 a[164:167], v42, s[28:31], s61 offen offset:2048 sc1\n\t"
    "v_mfma_f32_32x32x16_bf16 v[96:111], v[54:57], a[4:7], 0\n\t"
    "ds_read_b64_tr_b16 v[140:141], v16 offset:0\n\t"
    "ds_read_b64_tr_b16 v[142:143], v16 offset:1024\n\t"
    "s_waitcnt vmcnt(11)\n\t"
    "v_mfma_f32_32x32x16_bf16 v[80:95], v[58:61], a[12:15], v[80:95]\n\t"
    "ds_read_b64_tr_b16 v[112:113], v14 offset:2048\n\t"
    "ds_read_b64_tr_b16 v[114:115], v14 offset:3072\n\t"
    "buffer_load_dwordx4 a[172:175], v19, s[28:31], s62 offen offset:0 sc1\n\t"
    "v_mfma_f32_32x32x16_bf16 v[96:111], v[152:155], a[12:15], v[96:111]\n\t"
    "ds_read_b64_tr_b16 v[116:117], v16 offset:2048\n\t"
    "ds_read_b64_tr_b16 v[118:119], v16 offset:3072\n\t"
    "s_waitcnt vmcnt(11)\n\t"
    "v_mfma_f32_32x32x16_bf16 v[80:95], v[156:159], a[20:23], v[80:95]\n\t"
    "ds_read_b64_tr_b16 v[120:121], v6 offset:6144\n\t"
    "ds_read_b64_tr_b16 v[122:123], v6 offset:7168\n\t"
    "buffer_load_dwordx4 a[180:183], v19, s[28:31], s62 offen offset:1024 sc1\n\t"
    "v_mfma_f32_32x32x16_bf16 v[96:111], v[160:163], a[20:23], v[96:111]\n\t"
    "ds_read_b64_tr_b16 v[124:125], v13 offset:6144\n\t"
    "ds_read_b64_tr_b16 v[126:127], v13 offset:7168\n\t"
    "s_waitcnt vmcnt(11) lgkmcnt(10)\n\t"
    "v_mfma_f32_32x32x16_bf16 v[80:95], v[136:139], a[196:199], v[80:95]\n\t"
    "ds_read_b64_tr_b16 v[128:129], v6 offset:32768\n\t"
    "ds_read_b64_tr_b16 v[130:131], v6 offset:33792\n\t"
    "buffer_load_dwordx4 a[188:191], v19, s[28:31], s62 offen offset:2048 sc1\n\t"
    "s_waitcnt lgkmcnt(10)\n\t"
    "v_mfma_f32_32x32x16_bf16 v[96:111], v[140:143], a[196:199], v[96:111]\n\t"
    "ds_read_b64_tr_b16 v[132:133], v13 offset:32768\n\t"
    "ds_read_b64_tr_b16 v[134:135], v13 offset:33792\n\t"
    "s_waitcnt vmcnt(11) lgkmcnt(10)\n\t"
    "v_mfma_f32_32x32x16_bf16 v[80:95], v[112:115], a[204:207], v[80:95]\n\t"
    "ds_read_b64_tr_b16 v[136:137], v6 offset:34816\n\t"
    "ds_read_b64_tr_b16 v[138:139], v6 offset:35840\n\t"
    "buffer_load_dwordx4 a[212:215], v42, s[28:31], s62 offen offset:1024 sc1\n\t"
    "s_waitcnt lgkmcnt(10)\n\t"
    "v_mfma_f32_32x32x16_bf16 v[96:111], v[116:119], a[204:207], v[96:111]\n\t"
    "ds_read_b64_tr_b16 v[140:141], v13 offset:34816\n\t"
    "ds_read_b64_tr_b16 v[142:143], v13 offset:35840\n\t"
    "s_waitcnt vmcnt(11) lgkmcnt(10)\n\t"
    "v_mfma_f32_32x32x16_bf16 v[80:95], v[120:123], a[108:111], v[80:95]\n\t"
    "ds_read_b64_tr_b16 v[112:113], v6 offset:36864\n\t"
    "ds_read_b64_tr_b16 v[114:115], v6 offset:37888\n\t"
    "buffer_load_dwordx4 a[220:223], v42, s[28:31], s62 offen offset:2048 sc1\n\t"
    "s_waitcnt lgkmcnt(10)\n\t"
    "v_mfma_f32_32x32x16_bf16 v[96:111], v[124:127], a[108:111], v[96:111]\n\t"
    "ds_read_b64_tr_b16 v[116:117], v13 offset:36864\n\t"
    "ds_read_b64_tr_b16 v[118:119], v13 offset:37888\n\t"
    "s_waitcnt vmcnt(11) lgkmcnt(10)\n\t"
    "v_mfma_f32_32x32x16_bf16 v[80:95], v[128:131], a[116:119], v[80:95]\n\t"
    "ds_read_b64_tr_b16 v[120:121], v6 offset:38912\n\t"
    "ds_read_b64_tr_b16 v[122:123], v6 offset:39936\n\t"
    "buffer_load_dwordx4 a[28:31], v19, s[28:31], s59 offen offset:3072 sc1\n\t"
    "s_waitcnt lgkmcnt(10)\n\t"
    "v_mfma_f32_32x32x16_bf16 v[96:111], v[132:135], a[116:119], v[96:111]\n\t"
    "ds_read_b64_tr_b16 v[124:125], v13 offset:38912\n\t"
    "ds_read_b64_tr_b16 v[126:127], v13 offset:39936\n\t"
    "s_waitcnt vmcnt(11) lgkmcnt(10)\n\t"
    "v_mfma_f32_32x32x16_bf16 v[80:95], v[136:139], a[124:127], v[80:95]\n\t"
    "ds_read_b64_tr_b16 v[128:129], v40 offset:0\n\t"
    "ds_read_b64_tr_b16 v[130:131], v40 offset:1024\n\t"
    "buffer_load_dwordx4 a[36:39], v42, s[28:31], s59 offen offset:0 sc1\n\t"
    "s_waitcnt lgkmcnt(10)\n\t"
    "v_mfma_f32_32x32x16_bf16 v[96:111], v[140:143], a[124:127], v[96:111]\n\t"
    "ds_read_b64_tr_b16 v[132:133], v43 offset:0\n\t"
    "ds_read_b64_tr_b16 v[134:135], v43 offset:1024\n\t"
    "s_waitcnt vmcnt(11) lgkmcnt(10)\n\t"
    "v_mfma_f32_32x32x16_bf16 v[80:95], v[112:115], a[132:135], v[80:95]\n\t"
    "ds_read_b64_tr_b16 v[136:137], v40 offset:2048\n\t"
    "ds_read_b64_tr_b16 v[138:139], v40 offset:3072\n\t"
    "buffer_load_dwordx4 a[44:47], v42, s[28:31], s59 offen offset:1024 sc1\n\t"
    "s_waitcnt lgkmcnt(10)\n\t"
    "v_mfma_f32_32x32x16_bf16 v[96:111], v[116:119], a[132:135], v[96:111]\n\t"
    "ds_read_b64_tr_b16 v[140:141], v43 offset:2048\n\t"
    "ds_read_b64_tr_b16 v[142:143], v43 offset:3072\n\t"
    "s_waitcnt vmcnt(11) lgkmcnt(10)\n\t"
    "v_mfma_f32_32x32x16_bf16 v[80:95], v[120:123], a[140:143], v[80:95]\n\t"
    "ds_read_b64_tr_b16 v[112:113], v40 offset:4096\n\t"
    "ds_read_b64_tr_b16 v[114:115], v40 offset:5120\n\t"
    "buffer_load_dwordx4 a[52:55], v42, s[28:31], s59 offen offset:2048 sc1\n\t"
    "s_waitcnt lgkmcnt(10)\n\t"
    "v_mfma_f32_32x32x16_bf16 v[96:111], v[124:127], a[140:143], v[96:111]\n\t"
    "ds_read_b64_tr_b16 v[116:117], v43 offset:4096\n\t"
    "ds_read_b64_tr_b16 v[118:119], v43 offset:5120\n\t"
    "s_waitcnt vmcnt(11) lgkmcnt(10)\n\t"
    "v_mfma_f32_32x32x16_bf16 v[80:95], v[128:131], a[148:151], v[80:95]\n\t"
    "ds_read_b64_tr_b16 v[120:121], v6 offset:0\n\t"
    "ds_read_b64_tr_b16 v[122:123], v6 offset:1024\n\t"
    "buffer_load_dwordx4 a[60:63], v19, s[28:31], s60 offen offset:0 sc1\n\t"
    "s_waitcnt lgkmcnt(10)\n\t"
    "v_mfma_f32_32x32x16_bf16 v[96:111], v[132:135], a[148:151], v[96:111]\n\t"
    "ds_read_b64_tr_b16 v[124:125], v13 offset:0\n\t"
    "ds_read_b64_tr_b16 v[126:127], v13 offset:1024\n\t"
    "s_waitcnt vmcnt(11) lgkmcnt(10)\n\t"
    "v_mfma_f32_32x32x16_bf16 v[80:95], v[136:139], a[156:159], v[80:95]\n\t"
    "ds_read_b64_tr_b16 v[128:129], v6 offset:2048\n\t"
    "ds_read_b64_tr_b16 v[130:131], v6 offset:3072\n\t"
    "buffer_load_dwordx4 a[68:71], v19, s[28:31], s60 offen offset:1024 sc1\n\t"
    "s_waitcnt lgkmcnt(10)\n\t"
    "v_mfma_f32_32x32x16_bf16 v[96:111], v[140:143], a[156:159], v[96:111]\n\t"
    "ds_read_b64_tr_b16 v[132:133], v13 offset:2048\n\t"
    "ds_read_b64_tr_b16 v[134:135], v13 offset:3072\n\t"
    "s_waitcnt vmcnt(11) lgkmcnt(10)\n\t"
    "v_mfma_f32_32x32x16_bf16 v[80:95], v[112:115], a[164:167], v[80:95]\n\t"
    "ds_read_b64_tr_b16 v[136:137], v6 offset:4096\n\t"
    "ds_read_b64_tr_b16 v[138:139], v6 offset:5120\n\t"
    "buffer_load_dwordx4 a[76:79], v19, s[28:31], s60 offen offset:2048 sc1\n\t"
    "s_waitcnt lgkmcnt(10)\n\t"
    "v_mfma_f32_32x32x16_bf16 v[96:111], v[116:119], a[164:167], v[96:111]\n\t"
    "ds_read_b64_tr_b16 v[140:141], v13 offset:4096\n\t"
    "ds_read_b64_tr_b16 v[142:143], v13 offset:5120\n\t"
    "s_waitcnt vmcnt(11) lgkmcnt(10)\n\t"
    "v_mfma_f32_32x32x16_bf16 v[80:95], v[120:123], a[172:175], v[80:95]\n\t"
    "ds_read_b64_tr_b16 v[112:113], v14 offset:4096\n\t"
    "ds_read_b64_tr_b16 v[114:115], v14 offset:5120\n\t"
    "buffer_load_dwordx4 a[84:87], v19, s[28:31], s60 offen offset:3072 sc1\n\t"
    "s_waitcnt lgkmcnt(10)\n\t"
    "v_mfma_f32_32x32x16_bf16 v[96:111], v[124:127], a[172:175], v[96:111]\n\t"
    "ds_read_b64_tr_b16 v[116:117], v16 offset:4096\n\t"
    "ds_read_b64_tr_b16 v[118:119], v16 offset:5120\n\t"
    "s_waitcnt vmcnt(11) lgkmcnt(10)\n\t"
    "v_mfma_f32_32x32x16_bf16 v[80:95], v[128:131], a[180:183], v[80:95]\n\t"
    "ds_read_b64_tr_b16 v[120:121], v14 offset:6144\n\t"
    "ds_read_b64_tr_b16 v[122:123], v14 offset:7168\n\t"
    "buffer_load_dwordx4 a[92:95], v42, s[28:31], s60 offen offset:0 sc1\n\t"
    "s_waitcnt lgkmcnt(10)\n\t"
    "v_mfma_f32_32x32x16_bf16 v[96:111], v[132:135], a[180:183], v[96:111]\n\t"
    "ds_read_b64_tr_b16 v[124:125], v16 offset:6144\n\t"
    "ds_read_b64_tr_b16 v[126:127], v16 offset:7168\n\t"
    "s_waitcnt vmcnt(11) lgkmcnt(10)\n\t"
    "v_mfma_f32_32x32x16_bf16 v[80:95], v[136:139], a[188:191], v[80:95]\n\t"
    "buffer_load_dwordx4 a[100:103], v42, s[28:31], s60 offen offset:1024 sc1\n\t"
    "s_waitcnt lgkmcnt(8)\n\t"
    "v_mfma_f32_32x32x16_bf16 v[96:111], v[140:143], a[188:191], v[96:111]\n\t"
    "s_waitcnt vmcnt(11) lgkmcnt(6)\n\t"
    "v_mfma_f32_32x32x16_bf16 v[80:95], v[112:115], a[212:215], v[80:95]\n\t"
    "buffer_store_dwordx4 v[36:39], v144, s[12:15], s49 offen\n\t"
    "s_waitcnt lgkmcnt(4)\n\t"
    "v_mfma_f32_32x32x16_bf16 v[96:111], v[116:119], a[212:215], v[96:111]\n\t"
    "s_waitcnt vmcnt(11) lgkmcnt(2)\n\t"
    "v_mfma_f32_32x32x16_bf16 v[80:95], v[120:123], a[220:223], v[80:95]\n\t"
    "s_waitcnt lgkmcnt(0)\n\t"
    "v_mfma_f32_32x32x16_bf16 v[96:111], v[124:127], a[220:223], v[96:111]\n\t"
    "s_waitcnt vmcnt(10)\n\t"
    "v_mfma_f32_32x32x16_bf16 v[80:95], v[164:167], a[28:31], v[80:95]\n\t"
    "v_mfma_f32_32x32x16_bf16 v[96:111], v[168:171], a[28:31], v[96:111]\n\t"
    "s_waitcnt vmcnt(9)\n\t"
    "v_mfma_f32_32x32x16_bf16 v[80:95], v[172:175], a[36:39], v[80:95]\n\t"
    "v_mfma_f32_32x32x16_bf16 v[96:111], v[176:179], a[36:39], v[96:111]\n\t"
    "s_waitcnt vmcnt(8)\n\t"
    "v_mfma_f32_32x32x16_bf16 v[80:95], v[180:183], a[44:47], v[80:95]\n\t"
    "v_mfma_f32_32x32x16_bf16 v[96:111], v[184:187], a[44:47], v[96:111]\n\t"
    "s_waitcnt vmcnt(7)\n\t"
    "v_mfma_f32_32x32x16_bf16 v[80:95], v[188:191], a[52:55], v[80:95]\n\t"
    "v_mfma_f32_32x32x16_bf16 v[96:111], v[192:195], a[52:55], v[96:111]\n\t"
    "s_waitcnt vmcnt(6)\n\t"
    "v_mfma_f32_32x32x16_bf16 v[80:95], v[196:199], a[60:63], v[80:95]\n\t"
    "v_mfma_f32_32x32x16_bf16 v[96:111], v[200:203], a[60:63], v[96:111]\n\t"
    "s_waitcnt vmcnt(5)\n\t"
    "v_mfma_f32_32x32x16_bf16 v[80:95], v[204:207], a[68:71], v[80:95]\n\t"
    "v_mfma_f32_32x32x16_bf16 v[96:111], v[208:211], a[68:71], v[96:111]\n\t"
    "s_waitcnt vmcnt(4)\n\t"
    "v_mfma_f32_32x32x16_bf16 v[80:95], v[212:215], a[76:79], v[80:95]\n\t"
    "v_mfma_f32_32x32x16_bf16 v[96:111], v[216:219], a[76:79], v[96:111]\n\t"
    "s_waitcnt vmcnt(3)\n\t"
    "v_mfma_f32_32x32x16_bf16 v[80:95], v[220:223], a[84:87], v[80:95]\n\t"
    "v_mfma_f32_32x32x16_bf16 v[96:111], v[224:227], a[84:87], v[96:111]\n\t"
    "s_waitcnt vmcnt(2)\n\t"
    "v_mfma_f32_32x32x16_bf16 v[80:95], v[228:231], a[92:95], v[80:95]\n\t"
    "v_mfma_f32_32x32x16_bf16 v[96:111], v[232:235], a[92:95], v[96:111]\n\t"
    "s_waitcnt vmcnt(1)\n\t"
    "v_mfma_f32_32x32x16_bf16 v[96:111], v[240:243], a[100:103], v[96:111]\n\t"
    "v_mfma_f32_32x32x16_bf16 v[80:95], v[236:239], a[100:103], v[80:95]\n\t"
    "s_nop 10\n\t"
    "ds_write_b128 v5, v[104:107] offset:2048\n\t"
    "ds_write_b128 v5, v[108:111] offset:3072\n\t"
    "ds_write_b128 v5, v[96:99] offset:0\n\t"
    "ds_write_b128 v5, v[100:103] offset:1024\n\t"
    "ds_write_b128 v5, v[88:91] offset:38912\n\t"
    "ds_write_b128 v5, v[92:95] offset:39936\n\t"
    "v_lshlrev_b32 v7, 16, v46\n\t"
    "v_and_b32 v8, 0xffff0000, v46\n\t"
    "v_add_f32 v80, v80, v7\n\t"
    "v_add_f32 v81, v81, v8\n\t"
    "v_lshlrev_b32 v7, 16, v47\n\t"
    "v_and_b32 v8, 0xffff0000, v47\n\t"
    "v_add_f32 v82, v82, v7\n\t"
    "v_add_f32 v83, v83, v8\n\t"
    "v_lshlrev_b32 v7, 16, v48\n\t"
    "v_and_b32 v8, 0xffff0000, v48\n\t"
    "v_add_f32 v84, v84, v7\n\t"
    "v_add_f32 v85, v85, v8\n\t"
    "v_lshlrev_b32 v7, 16, v49\n\t"
    "v_and_b32 v8, 0xffff0000, v49\n\t"
    "v_add_f32 v86, v86, v7\n\t"
    "v_add_f32 v87, v87, v8\n\t"
    "s_waitcnt lgkmcnt(0)\n\t"
    "s_barrier\n\t"
    "ds_read_b128 v[96:99], v44 offset:38912\n\t"
    "ds_read_b128 v[100:103], v62 offset:0\n\t"
    "ds_read_b128 v[104:107], v63 offset:2048\n\t"
    "ds_read_b128 v[108:111], v44 offset:39936\n\t"
    "ds_read_b128 v[88:91], v62 offset:1024\n\t"
    "ds_read_b128 v[92:95], v63 offset:3072\n\t"
    "s_waitcnt lgkmcnt(4)\n\t"
    "v_pk_add_f32 v[96:97], v[96:97], v[100:101]\n\t"
    "s_waitcnt lgkmcnt(3)\n\t"
    "v_pk_add_f32 v[80:81], v[80:81], v[104:105]\n\t"
    "v_pk_add_f32 v[98:99], v[98:99], v[102:103]\n\t"
    "v_pk_add_f32 v[82:83], v[82:83], v[106:107]\n\t"
    "v_pk_add_f32 v[80:81], v[80:81], v[96:97]\n\t"
    "v_pk_add_f32 v[82:83], v[82:83], v[98:99]\n\t"
    "v_cvt_pk_bf16_f32 v46, v80, v81\n\t"
    "v_cvt_pk_bf16_f32 v47, v82, v83\n\t"
    "s_waitcnt lgkmcnt(1)\n\t"
    "v_pk_add_f32 v[108:109], v[108:109], v[88:89]\n\t"
    "s_waitcnt lgkmcnt(0)\n\t"
    "v_pk_add_f32 v[84:85], v[84:85], v[92:93]\n\t"
    "v_pk_add_f32 v[110:111], v[110:111], v[90:91]\n\t"
    "v_pk_add_f32 v[86:87], v[86:87], v[94:95]\n\t"
    "v_pk_add_f32 v[84:85], v[84:85], v[108:109]\n\t"
    "v_pk_add_f32 v[86:87], v[86:87], v[110:111]\n\t"
    "v_cvt_pk_bf16_f32 v48, v84, v85\n\t"
    "v_cvt_pk_bf16_f32 v49, v86, v87\n\t"
    "s_nop 0\n\t"
    "v_permlane32_swap_b32 v46, v48\n\t"
    "v_permlane32_swap_b32 v47, v49\n\t"
    "buffer_store_dwordx4 v[46:49], v144, s[12:15], s44 offen\n\t"
    "L_end_%=:\n\t"
    :
    : [ka] "s"(__builtin_amdgcn_kernarg_segment_ptr()), [bid] "s"(blockIdx.x), [tid] "{v0}"(threadIdx.x),
      [lds] "s"((unsigned)(size_t)smem)
    : "memory", "scc", "vcc", "v1", "v2", "v3", "v4", "v5", "v6", "v7", "v8", "v9", "v10", "v11", "v12", "v13", "v14", "v15", "v16", "v17", "v18", "v19", "v20", "v21", "v22", "v23", "v24", "v25", "v26", "v27", "v28", "v29", "v30", "v31", "v32", "v33", "v34", "v35", "v36", "v37", "v38", "v39", "v40", "v41", "v42", "v43", "v44", "v45", "v46", "v47", "v48", "v49", "v50", "v51", "v52", "v53", "v54", "v55", "v56", "v57", "v58", "v59", "v60", "v61", "v62", "v63", "v64", "v65", "v66", "v67", "v68", "v69", "v70", "v71", "v72", "v73", "v74", "v75", "v76", "v77", "v78", "v79", "v80", "v81", "v82", "v83", "v84", "v85", "v86", "v87", "v88", "v89", "v90", "v91", "v92", "v93", "v94", "v95", "v96", "v97", "v98", "v99", "v100", "v101", "v102", "v103", "v104", "v105", "v106", "v107", "v108", "v109", "v110", "v111", "v112", "v113", "v114", "v115", "v116", "v117", "v118", "v119", "v120", "v121", "v122", "v123", "v124", "v125", "v126", "v127", "v128", "v129", "v130", "v131", "v132", "v133", "v134", "v135", "v136", "v137", "v138", "v139", "v140", "v141", "v142", "v143", "v144", "v145", "v146", "v147", "v148", "v149", "v150", "v151", "v152", "v153", "v154", "v155", "v156", "v157", "v158", "v159", "v160", "v161", "v162", "v163", "v164", "v165", "v166", "v167", "v168", "v169", "v170", "v171", "v172", "v173", "v174", "v175", "v176", "v177", "v178", "v179", "v180", "v181", "v182", "v183", "v184", "v185", "v186", "v187", "v188", "v189", "v190", "v191", "v192", "v193", "v194", "v195", "v196", "v197", "v198", "v199", "v200", "v201", "v202", "v203", "v204", "v205", "v206", "v207", "v208", "v209", "v210", "v211", "v212", "v213", "v214", "v215", "v216", "v217", "v218", "v219", "v220", "v221", "v222", "v223", "v224", "v225", "v226", "v227", "v228", "v229", "v230", "v231", "v232", "v233", "v234", "v235", "v236", "v237", "v238", "v239", "v240", "v241", "v242", "v243", "v244", "v245", "v246", "v247", "v248", "v249", "v250", "v251", "v252", "v253", "v254", "v255", "a0", "a1", "a2", "a3", "a4", "a5", "a6", "a7", "a8", "a9", "a10", "a11", "a12", "a13", "a14", "a15", "a16", "a17", "a18", "a19", "a20", "a21", "a22", "a23", "a24", "a25", "a26", "a27", "a28", "a29", "a30", "a31", "a32", "a33", "a34", "a35", "a36", "a37", "a38", "a39", "a40", "a41", "a42", "a43", "a44", "a45", "a46", "a47", "a48", "a49", "a50", "a51", "a52", "a53", "a54", "a55", "a56", "a57", "a58", "a59", "a60", "a61", "a62", "a63", "a64", "a65", "a66", "a67", "a68", "a69", "a70", "a71", "a72", "a73", "a74", "a75", "a76", "a77", "a78", "a79", "a80", "a81", "a82", "a83", "a84", "a85", "a86", "a87", "a88", "a89", "a90", "a91", "a92", "a93", "a94", "a95", "a96", "a97", "a98", "a99", "a100", "a101", "a102", "a103", "a104", "a105", "a106", "a107", "a108", "a109", "a110", "a111", "a112", "a113", "a114", "a115", "a116", "a117", "a118", "a119", "a120", "a121", "a122", "a123", "a124", "a125", "a126", "a127", "a128", "a129", "a130", "a131", "a132", "a133", "a134", "a135", "a136", "a137", "a138", "a139", "a140", "a141", "a142", "a143", "a144", "a145", "a146", "a147", "a148", "a149", "a150", "a151", "a152", "a153", "a154", "a155", "a156", "a157", "a158", "a159", "a160", "a161", "a162", "a163", "a164", "a165", "a166", "a167", "a168", "a169", "a170", "a171", "a172", "a173", "a174", "a175", "a176", "a177", "a178", "a179", "a180", "a181", "a182", "a183", "a184", "a185", "a186", "a187", "a188", "a189", "a190", "a191", "a192", "a193", "a194", "a195", "a196", "a197", "a198", "a199", "a200", "a201", "a202", "a203", "a204", "a205", "a206", "a207", "a208", "a209", "a210", "a211", "a212", "a213", "a214", "a215", "a216", "a217", "a218", "a219", "a220", "a221", "a222", "a223", "a224", "a225", "a226", "a227", "a228", "a229", "a230", "a231", "a232", "a233", "a234", "a235", "a236", "a237", "a238", "a239", "a240", "a241", "a242", "a243", "a244", "a245", "a246", "a247", "a248", "a249", "a250", "a251", "a252", "a253", "a254", "a255", "s8", "s9", "s10", "s11", "s12", "s13", "s14", "s15", "s16", "s17", "s18", "s19", "s20", "s21", "s22", "s23", "s24", "s25", "s26", "s27", "s28", "s29", "s30", "s31", "s33", "s34", "s35", "s36", "s37", "s38", "s39", "s40", "s41", "s42", "s43", "s44", "s45", "s46", "s47", "s48", "s49", "s50", "s51", "s52", "s53", "s54", "s55", "s56", "s57", "s58", "s59", "s60", "s61", "s62", "s63", "s64", "s65", "s66", "s67", "s68", "s69", "s70", "s71", "s72", "s73", "s74", "s75", "s76", "s77", "s78", "s79", "s80", "s81", "s82", "s83", "s84", "s85", "s86", "s87", "s88", "s89", "s90", "s91", "s92", "s93", "s94", "s95", "s96", "s97", "s98", "s99");
}

extern "C" void kernel_launch(void* const* d_in, const int* in_sizes, int n_in, void* d_out, int out_size, void* d_ws,
                              size_t ws_size, hipStream_t stream) {
  fused_block_kernel<<<256, 256, 0, stream>>>(d_in[0], d_in[1], d_in[2], d_in[4], d_in[5], d_in[6], d_out, d_ws);
}
```
